```python
import torch

def _softmax(x, axis=-1):
    return torch.softmax(x, dim=axis)
def _gelu(x):
    return torch.nn.functional.gelu(x, approximate="tanh")
def _silu(x):
    return torch.nn.functional.silu(x)
def _sigmoid(x):
    return torch.sigmoid(x)
def _layernorm(x, axis=-1, eps=1e-5):
    m = x.mean(dim=axis, keepdim=True)
    v = x.var(dim=axis, keepdim=True, unbiased=False)
    return (x - m) / torch.sqrt(v + eps)
def _scatter_add(tgt, idx, src, axis=0):
    return tgt.clone().index_add_(axis, idx, src)
def _segment_sum(src, seg, n):
    out = torch.zeros((n,) + src.shape[1:], dtype=src.dtype)
    return out.index_add_(0, seg, src)
def _pad(x, pw):
    flat = tuple(v for pair in reversed(pw) for v in pair)
    return torch.nn.functional.pad(x, flat)

kernel_name = 'gated_mix__compressed_kv_cla__dense_activation_silu__decode'

def setup_inputs():

    _w = (lambda a: (lambda t: t.to(torch.bfloat16) if t.is_floating_point() else t)(torch.as_tensor(a)))
    def _A_setup():
        B, H, Sq, Sk, D, M = 1, 32, 1, 4096, 128, 640
        Dc = 4 * D
        return {
            "x": _w((lambda *s: torch.randn(*s, dtype=torch.bfloat16) * 0.05)(B, Sq, M)),
            "c_kv": _w((lambda *s: torch.randn(*s, dtype=torch.bfloat16) * 0.2)(B, Sk, Dc)),
            "w_q": _w((lambda *s: torch.randn(*s, dtype=torch.bfloat16) * 3.2)(M, H, D) * (M ** -0.5)),
            "w_uk": _w((lambda *s: torch.randn(*s, dtype=torch.bfloat16) * 3.2)(Dc, H, D) * (Dc ** -0.5)),
            "w_uv": _w((lambda *s: torch.randn(*s, dtype=torch.bfloat16) * 0.2)(Dc, H, D) * (Dc ** -0.5)),
            "w_o": _w((lambda *s: torch.randn(*s, dtype=torch.bfloat16) * 0.2)(H, D, M) * ((H * D) ** -0.5)),
        }
    _A_in = _A_setup()

    def _B_setup():
        B, S, D, F = 1, 1, 640, 1728
        return {
            "x": _w((lambda *s: torch.randn(*s, dtype=torch.bfloat16) * 0.05)(B, S, D)),
            "w1": _w((lambda *s: torch.randn(*s, dtype=torch.bfloat16) * 0.05)(D, F) * (D ** -0.5)),
            "w2": _w((lambda *s: torch.randn(*s, dtype=torch.bfloat16) * 0.05)(F, D) * (F ** -0.5)),
        }
    _B_in = _B_setup()

    return {
        "A_x": _A_in["x"],
        "A_c_kv": _A_in["c_kv"],
        "A_w_q": _A_in["w_q"],
        "A_w_uk": _A_in["w_uk"],
        "A_w_uv": _A_in["w_uv"],
        "A_w_o": _A_in["w_o"],
        "B_x": _B_in["x"],
        "B_w1": _B_in["w1"],
        "B_w2": _B_in["w2"],
        "gate_w": _w((lambda *s: torch.randn(*s, dtype=torch.bfloat16) * 0.05)(640) * 16.0),
    }

def reference(A_x, A_c_kv, A_w_q, A_w_uk, A_w_uv, A_w_o, B_x, B_w1, B_w2, gate_w):
    def _A_ref(x, c_kv, w_q, w_uk, w_uv, w_o):
        B, Sq, M = x.shape[0], x.shape[1], x.shape[2]
        Sk = c_kv.shape[1]
        H, D = w_q.shape[1], w_q.shape[2]
        q = torch.einsum("bqm,mhd->bhqd", x, w_q)
        k = torch.einsum("bkc,chd->bhkd", c_kv, w_uk)
        v = torch.einsum("bkc,chd->bhkd", c_kv, w_uv)
        scores = torch.einsum("bhqd,bhkd->bhqk", q, k) * (D ** -0.5)
        off = Sk - Sq
        qi = torch.arange(Sq)[:, None]
        kj = torch.arange(Sk)[None, :]
        cmask = kj <= (qi + off)
        scores = torch.where(cmask[None, None], scores, -1e9)
        p = _softmax(scores, axis=-1)
        ctx = torch.einsum("bhqk,bhkd->bhqd", p, v)
        return torch.einsum("bhqd,hdm->bqm", ctx, w_o)

    def _B_ref(x, w1, w2):
        h = torch.einsum("bsd,df->bsf", x, w1)
        h = _silu(h)
        out = torch.einsum("bsf,fd->bsd", h, w2)
        return out

    x = A_x
    g = _sigmoid((x * gate_w).sum(-1, keepdims=True))
    a = _A_ref(x, A_c_kv, A_w_q, A_w_uk, A_w_uv, A_w_o)
    b = _B_ref(x, B_w1, B_w2)
    return g * a + (1 - g) * b
```

```cpp
#include <hip/hip_runtime.h>
#include <hip/hip_bf16.h>

typedef unsigned short u16;
__device__ __forceinline__ float bf2f(u16 b) { return __uint_as_float(((unsigned)b) << 16); }
__device__ __forceinline__ u16 f2bf(float f) { unsigned u = __float_as_uint(f); return (u16)((u + 0x7FFFu + ((u >> 16) & 1u)) >> 16); }
__device__ __forceinline__ float rbf(float f) { return bf2f(f2bf(f)); }

constexpr int H = 32, SK = 4096, D = 128, M = 640, DC = 512, F = 1728;
constexpr int NSPLIT = 64, KPS = SK / NSPLIT;

constexpr size_t O_Q = 0;
constexpr size_t O_HH = O_Q + 4096;
constexpr size_t O_QA = O_HH + 2048;
constexpr size_t O_ML = O_QA + H * DC;
constexpr size_t O_PO = O_ML + NSPLIT * H * 2;
constexpr size_t O_CTX = O_PO + (size_t)NSPLIT * H * DC;

__global__ void k1(const u16* x, const u16* wq, const u16* w1, float* ws) {
  __shared__ float xs[M];
  for (int i = threadIdx.x; i < M; i += blockDim.x) xs[i] = bf2f(x[i]);
  __syncthreads();
  int n = blockIdx.x * blockDim.x + threadIdx.x;
  if (n < H * D) {
    float acc = 0.f;
    for (int m = 0; m < M; ++m) acc += xs[m] * bf2f(wq[(size_t)m * (H * D) + n]);
    ws[O_Q + n] = rbf(acc);
  } else if (n < H * D + F) {
    int f = n - H * D;
    float acc = 0.f;
    for (int m = 0; m < M; ++m) acc += xs[m] * bf2f(w1[(size_t)m * F + f]);
    acc = rbf(acc);
    float s = acc / (1.f + __expf(-acc));
    ws[O_HH + f] = rbf(s);
  }
}
__global__ void k2(const u16* wuk, float* ws) {
  int w = (blockIdx.x * blockDim.x + threadIdx.x) >> 6, lane = threadIdx.x & 63;
  int c = w / H, h = w % H;
  const u16* p = wuk + ((size_t)c * H + h) * D + lane * 2;
  float acc = bf2f(p[0]) * ws[O_Q + h * D + lane * 2] + bf2f(p[1]) * ws[O_Q + h * D + lane * 2 + 1];
  for (int o = 32; o; o >>= 1) acc += __shfl_xor(acc, o);
  if (lane == 0) ws[O_QA + h * DC + c] = acc * 0.08838834764831845f;
}
__global__ void k3(const u16* ckv, float* ws) {
  __shared__ float qa[DC];
  __shared__ float sc[KPS];
  int s = blockIdx.x, h = blockIdx.y, tid = threadIdx.x;
  for (int i = tid; i < DC; i += blockDim.x) qa[i] = ws[O_QA + h * DC + i];
  __syncthreads();
  int wave = tid >> 6, lane = tid & 63;
  for (int k = wave; k < KPS; k += 4) {
    const u16* row = ckv + (size_t)(s * KPS + k) * DC;
    float acc = 0.f;
    for (int j = 0; j < 8; ++j) acc += qa[lane * 8 + j] * bf2f(row[lane * 8 + j]);
    for (int o = 32; o; o >>= 1) acc += __shfl_xor(acc, o);
    if (lane == 0) sc[k] = acc;
  }
  __syncthreads();
  float m = -INFINITY;
  for (int k = 0; k < KPS; ++k) m = fmaxf(m, sc[k]);
  float l = 0.f;
  float a0 = 0.f, a1 = 0.f;
  for (int k = 0; k < KPS; ++k) {
    float p = __expf(sc[k] - m);
    l += p;
    const u16* row = ckv + (size_t)(s * KPS + k) * DC;
    a0 += p * bf2f(row[tid * 2]);
    a1 += p * bf2f(row[tid * 2 + 1]);
  }
  float* po = ws + O_PO + ((size_t)s * H + h) * DC;
  po[tid * 2] = a0; po[tid * 2 + 1] = a1;
  if (tid == 0) { ws[O_ML + (s * H + h) * 2] = m; ws[O_ML + (s * H + h) * 2 + 1] = l; }
}
__global__ void k4(const u16* wuv, float* ws) {
  __shared__ float cl[DC];
  int h = blockIdx.x, tid = threadIdx.x;
  float M_ = -INFINITY;
  for (int s = 0; s < NSPLIT; ++s) M_ = fmaxf(M_, ws[O_ML + (s * H + h) * 2]);
  float L = 0.f;
  for (int s = 0; s < NSPLIT; ++s) L += __expf(ws[O_ML + (s * H + h) * 2] - M_) * ws[O_ML + (s * H + h) * 2 + 1];
  for (int c = tid; c < DC; c += blockDim.x) {
    float a = 0.f;
    for (int s = 0; s < NSPLIT; ++s) a += __expf(ws[O_ML + (s * H + h) * 2] - M_) * ws[O_PO + ((size_t)s * H + h) * DC + c];
    cl[c] = a / L;
  }
  __syncthreads();
  if (tid < D) {
    float a = 0.f;
    for (int c = 0; c < DC; ++c) a += cl[c] * bf2f(wuv[((size_t)c * H + h) * D + tid]);
    ws[O_CTX + h * D + tid] = rbf(a);
  }
}
__global__ void k5(const u16* x, const u16* gw, const u16* wo, const u16* w2, const float* ws, u16* out) {
  __shared__ float red[256];
  int m = blockIdx.x, tid = threadIdx.x;
  float a = 0.f, b = 0.f, g = 0.f;
  for (int n = tid; n < H * D; n += 256) a += ws[O_CTX + n] * bf2f(wo[(size_t)n * M + m]);
  for (int f = tid; f < F; f += 256) b += ws[O_HH + f] * bf2f(w2[(size_t)f * M + m]);
  for (int i = tid; i < M; i += 256) g += rbf(bf2f(x[i]) * bf2f(gw[i]));
  float v[3] = {a, b, g};
  float r[3];
  for (int j = 0; j < 3; ++j) {
    red[tid] = v[j]; __syncthreads();
    for (int o = 128; o; o >>= 1) { if (tid < o) red[tid] += red[tid + o]; __syncthreads(); }
    r[j] = red[0]; __syncthreads();
  }
  if (tid == 0) {
    float gg = rbf(r[2]);
    gg = rbf(1.f / (1.f + __expf(-gg)));
    float aa = rbf(r[0]), bb = rbf(r[1]);
    float o = rbf(gg * aa) + rbf(rbf(1.f - gg) * bb);
    out[m] = f2bf(o);
  }
}

extern "C" void kernel_launch(void* const* d_in, const int* in_sizes, int n_in,
                              void* d_out, int out_size, void* d_ws, size_t ws_size,
                              hipStream_t stream) {
  const u16* x = (const u16*)d_in[0];
  const u16* ckv = (const u16*)d_in[1];
  const u16* wq = (const u16*)d_in[2];
  const u16* wuk = (const u16*)d_in[3];
  const u16* wuv = (const u16*)d_in[4];
  const u16* wo = (const u16*)d_in[5];
  const u16* w1 = (const u16*)d_in[7];
  const u16* w2 = (const u16*)d_in[8];
  const u16* gw = (const u16*)d_in[9];
  float* ws = (float*)d_ws;
  k1<<<(H * D + F + 255) / 256, 256, 0, stream>>>(x, wq, w1, ws);
  k2<<<(DC * H * 64) / 256, 256, 0, stream>>>(wuk, ws);
  k3<<<dim3(NSPLIT, H), 256, 0, stream>>>(ckv, ws);
  k4<<<H, 256, 0, stream>>>(wuv, ws);
  k5<<<M, 256, 0, stream>>>(x, gw, wo, w2, ws, (u16*)d_out);
}
```

```cpp
#include <hip/hip_runtime.h>
#include <hip/hip_bf16.h>
typedef unsigned short u16;
#define GAS __attribute__((address_space(1)))
typedef GAS unsigned long long gu64;
#define RLX_AGENT __ATOMIC_RELAXED, __HIP_MEMORY_SCOPE_AGENT
__device__ __forceinline__ float bf2f(u16 b) { return __uint_as_float(((unsigned)b) << 16); }
__device__ __forceinline__ u16 f2bf(float f) { unsigned u = __float_as_uint(f); return (u16)((u + 0x7FFFu + ((u >> 16) & 1u)) >> 16); }
__device__ __forceinline__ float rbf(float f) { return bf2f(f2bf(f)); }
constexpr int H = 32, SK = 4096, D = 128, M = 640, DC = 512, F = 1728;
constexpr int NSPLIT = 64, KPS = SK / NSPLIT;
constexpr size_t O_Q = 16384;
constexpr size_t O_HH = O_Q + 4096;
constexpr size_t O_QA = O_HH + 2048;
constexpr size_t O_ML = O_QA + H * DC;
constexpr size_t O_PO = O_ML + NSPLIT * H * 2;
constexpr size_t O_CTX = O_PO + (size_t)NSPLIT * H * DC;
constexpr int NWG = 256, NT = 512;

__device__ __forceinline__ bool gbar(gu64* flags, int k, unsigned long long tag) {
  __syncthreads();
  __shared__ int okf;
  if (threadIdx.x == 0) { __threadfence(); asm volatile("s_waitcnt vmcnt(0)" ::: "memory"); __hip_atomic_store(flags + k * NWG + blockIdx.x, tag, RLX_AGENT); okf = 1; }
  __syncthreads();
  if (threadIdx.x < NWG) {
    unsigned spins = 0;
    while (__hip_atomic_load(flags + k * NWG + threadIdx.x, RLX_AGENT) != tag) { if (++spins > 200000) { okf = 0; break; } __builtin_amdgcn_s_sleep(1); }
    __threadfence();
    asm volatile("s_waitcnt vmcnt(0)" ::: "memory");
  }
  __syncthreads();
  return okf;
}

__global__ void __launch_bounds__(NT) fused(const u16* x, const u16* ckv, const u16* wq, const u16* wuk, const u16* wuv, const u16* wo, const u16* w1, const u16* w2, const u16* gw, float* ws, u16* out) {
  __shared__ float sm[2048];
  gu64* flags = (gu64*)ws;
  const unsigned long long e = __hip_atomic_load(flags + 2047, RLX_AGENT);
  const unsigned long long tag = e * 6364136223846793005ull + 1442695040888963407ull;
  const int tid = threadIdx.x, wg = blockIdx.x, lane = tid & 63, wave = tid >> 6;
  for (int i = tid; i < M; i += NT) sm[i] = bf2f(x[i]);
  __syncthreads();
  {
    for (int n = wg * 23 + wave; n < wg * 23 + 23 && n < H * D + F; n += 8) {
      float acc = 0.f;
      if (n < H * D) { for (int m = lane; m < M; m += 64) acc += sm[m] * bf2f(wq[(size_t)m * (H * D) + n]); }
      else { for (int m = lane; m < M; m += 64) acc += sm[m] * bf2f(w1[(size_t)m * F + (n - H * D)]); }
      for (int o = 32; o; o >>= 1) acc += __shfl_xor(acc, o);
      if (lane == 0) { if (n < H * D) ws[O_Q + n] = rbf(acc); else { acc = rbf(acc); ws[O_HH + n - H * D] = rbf(acc / (1.f + __expf(-acc))); } }
    }
  }
  if (!gbar(flags, 0, tag)) return;
  for (int i = 0; i < 8; ++i) {
    int w = wg * 64 + wave * 8 + i; int c = w / H, h = w % H;
    const u16* p = wuk + ((size_t)c * H + h) * D + lane * 2;
    float acc = bf2f(p[0]) * ws[O_Q + h * D + lane * 2] + bf2f(p[1]) * ws[O_Q + h * D + lane * 2 + 1];
    for (int o = 32; o; o >>= 1) acc += __shfl_xor(acc, o);
    if (lane == 0) ws[O_QA + h * DC + c] = acc * 0.08838834764831845f;
  }
  if (!gbar(flags, 1, tag)) return;
  {
    int item = wg * 8 + wave; int s = item / H, h = item % H;
    float qa[8]; for (int j = 0; j < 8; ++j) qa[j] = ws[O_QA + h * DC + lane * 8 + j];
    float m = -INFINITY, l = 0.f, a[8] = {0, 0, 0, 0, 0, 0, 0, 0};
    for (int k = 0; k < KPS; ++k) {
      const uint4 rv = *(const uint4*)(ckv + (size_t)(s * KPS + k) * DC + lane * 8);
      float c8[8] = {__uint_as_float(rv.x << 16), __uint_as_float(rv.x & 0xffff0000u), __uint_as_float(rv.y << 16), __uint_as_float(rv.y & 0xffff0000u), __uint_as_float(rv.z << 16), __uint_as_float(rv.z & 0xffff0000u), __uint_as_float(rv.w << 16), __uint_as_float(rv.w & 0xffff0000u)};
      float acc = 0.f; for (int j = 0; j < 8; ++j) acc += qa[j] * c8[j];
      for (int o = 32; o; o >>= 1) acc += __shfl_xor(acc, o);
      float mn = fmaxf(m, acc), al = __expf(m - mn), p = __expf(acc - mn);
      l = l * al + p; for (int j = 0; j < 8; ++j) a[j] = a[j] * al + p * c8[j];
      m = mn;
    }
    float* po = ws + O_PO + ((size_t)s * H + h) * DC + lane * 8;
    for (int j = 0; j < 8; ++j) po[j] = a[j];
    if (lane == 0) { ws[O_ML + (s * H + h) * 2] = m; ws[O_ML + (s * H + h) * 2 + 1] = l; }
  }
  if (!gbar(flags, 2, tag)) return;
  {
    int h = wg >> 3, j = wg & 7;
    float M_ = -INFINITY;
    for (int s = 0; s < NSPLIT; ++s) M_ = fmaxf(M_, ws[O_ML + (s * H + h) * 2]);
    float L = 0.f;
    for (int s = 0; s < NSPLIT; ++s) L += __expf(ws[O_ML + (s * H + h) * 2] - M_) * ws[O_ML + (s * H + h) * 2 + 1];
    float a = 0.f;
    for (int s = 0; s < NSPLIT; ++s) a += __expf(ws[O_ML + (s * H + h) * 2] - M_) * ws[O_PO + ((size_t)s * H + h) * DC + tid];
    sm[tid] = a / L;
    __syncthreads();
    int d = tid & 15, cp = tid >> 4; float acc = 0.f;
    for (int c = cp * 16; c < cp * 16 + 16; ++c) acc += sm[c] * bf2f(wuv[((size_t)c * H + h) * D + j * 16 + d]);
    sm[1024 + tid] = acc;
    __syncthreads();
    if (tid < 16) { float t = 0.f; for (int p = 0; p < 32; ++p) t += sm[1024 + p * 16 + tid]; ws[O_CTX + h * D + j * 16 + tid] = rbf(t); }
  }
  if (!gbar(flags, 3, tag)) return;
  for (int m = wg; m < M; m += NWG) {
    float a = 0.f, b = 0.f, g = 0.f;
    for (int n = tid; n < H * D; n += NT) a += ws[O_CTX + n] * bf2f(wo[(size_t)n * M + m]);
    for (int f = tid; f < F; f += NT) b += ws[O_HH + f] * bf2f(w2[(size_t)f * M + m]);
    for (int i = tid; i < M; i += NT) g += rbf(bf2f(x[i]) * bf2f(gw[i]));
    for (int o = 32; o; o >>= 1) { a += __shfl_xor(a, o); b += __shfl_xor(b, o); g += __shfl_xor(g, o); }
    __syncthreads();
    if (lane == 0) { sm[wave] = a; sm[8 + wave] = b; sm[16 + wave] = g; }
    __syncthreads();
    if (tid == 0) {
      float aa = 0, bb = 0, gg = 0; for (int w = 0; w < 8; ++w) { aa += sm[w]; bb += sm[8 + w]; gg += sm[16 + w]; }
      gg = rbf(gg); gg = rbf(1.f / (1.f + __expf(-gg))); aa = rbf(aa); bb = rbf(bb);
      out[m] = f2bf(rbf(gg * aa) + rbf(rbf(1.f - gg) * bb));
    }
  }
  if (wg == 0 && tid == 0) __hip_atomic_store(flags + 2047, e + 1, RLX_AGENT);
}

extern "C" void kernel_launch(void* const* d_in, const int* in_sizes, int n_in,
                              void* d_out, int out_size, void* d_ws, size_t ws_size,
                              hipStream_t stream) {
  fused<<<NWG, NT, 0, stream>>>((const u16*)d_in[0], (const u16*)d_in[1], (const u16*)d_in[2], (const u16*)d_in[3], (const u16*)d_in[4], (const u16*)d_in[5], (const u16*)d_in[7], (const u16*)d_in[8], (const u16*)d_in[9], (float*)d_ws, (u16*)d_out);
}
```

```cpp
#include <hip/hip_runtime.h>
#include <hip/hip_bf16.h>

typedef unsigned short u16;
typedef unsigned u32;
typedef unsigned long long u64;
typedef unsigned char u8;
typedef u32 v4u __attribute__((ext_vector_type(4)));
typedef u32 v2u __attribute__((ext_vector_type(2)));
typedef float f4 __attribute__((ext_vector_type(4)));
typedef __bf16 bf8 __attribute__((ext_vector_type(8)));
typedef __bf16 bf2 __attribute__((ext_vector_type(2)));
typedef short s4 __attribute__((ext_vector_type(4)));
typedef short s8 __attribute__((ext_vector_type(8)));
#define LAS __attribute__((address_space(3)))
#define GAS __attribute__((address_space(1)))
typedef GAS u64 gu64;
#define RLX_AGENT __ATOMIC_RELAXED, __HIP_MEMORY_SCOPE_AGENT

constexpr int H = 32, SK = 4096, D = 128, M = 640, DC = 512, F = 1728;
constexpr int NFFN = 108, NRED = 40, NPART = 64 + NFFN;

constexpr u32 W_EPOCH = 0;
constexpr u32 W_TOKL = 4096;
constexpr u32 W_TOKA = 8192;
constexpr u32 W_QP = 16384;
constexpr u32 QA_REP = 32768 + 256;
constexpr u32 W_QA = W_QP + 32 * 4 * 128 * 8;
constexpr u32 W_ML = W_QA + 8 * QA_REP;
constexpr u32 W_OP = W_ML + 8 * 32 * 128;
constexpr u32 W_PART = W_OP + 32 * 32768;
constexpr u32 W_END = W_PART + NRED * NPART * 16 * 8;
#define WS_BASE 0

constexpr u32 L_WQ = 0, L_WUK = 40960, L_W1 = 73728, L_W2 = 94208, L_Q = 114688, L_RED = 115200, L_FIN = 116736;
constexpr u32 A_C = 0, A_QA = 131072, A_P = 147456, A_MX = 151552, A_SM = 152064;
constexpr u32 V_WUV = 0, V_WO = 65536, V_O = 147456, V_FLAG = 147584, V_CP = 147712;
constexpr u32 M_EV = 152576, M_PF = 152832;
constexpr u32 L_QA = 116224;
constexpr u32 L_GW = 119040, L_GATE = 120320;
constexpr u32 L_X = 117760;
constexpr u32 LDS_BYTES = 153600;

#define A_DELAY 0
#define V_DELAY 70
#define PREFETCH_BYTES 12288
#define C_SWAP 1
#define F_DELAY 10
#define C_NT 1
#define V_HINT 1
#define WUK_LATE 1
constexpr u32 SPIN_CAP = 20000;
constexpr u32 SENT = 0xFFFFFFFFu;

#define STAMP(i) do { } while (0)

__device__ __forceinline__ void dma16(u32 voff, const void* sbase, u32 ldsaddr) {
  asm volatile("s_mov_b32 m0, %2\n\ts_nop 0\n\tglobal_load_lds_dwordx4 %0, %1 nt" ::"v"(voff), "s"(sbase), "s"(ldsaddr) : "memory");
}
__device__ __forceinline__ void dma16d(u32 voff, const void* sbase, u32 ldsaddr) {
  asm volatile("s_mov_b32 m0, %2\n\ts_nop 0\n\tglobal_load_lds_dwordx4 %0, %1" ::"v"(voff), "s"(sbase), "s"(ldsaddr) : "memory");
}
__device__ __forceinline__ void dma16c(u32 voff, const void* sbase, u32 ldsaddr) {
  asm volatile("s_mov_b32 m0, %2\n\ts_nop 0\n\tglobal_load_lds_dwordx4 %0, %1 sc1" ::"v"(voff), "s"(sbase), "s"(ldsaddr) : "memory");
}
__device__ __forceinline__ void dma4(u32 voff, const void* sbase, u32 ldsaddr) {
  asm volatile("s_mov_b32 m0, %2\n\ts_nop 0\n\tglobal_load_lds_dword %0, %1" ::"v"(voff), "s"(sbase), "s"(ldsaddr) : "memory");
}
#define WAITVM(n) asm volatile("s_waitcnt vmcnt(" #n ")" ::: "memory")
#define PIN() __builtin_amdgcn_sched_barrier(0)
#define RELOAD() asm volatile("" ::: "memory")
#define BAR() do { asm volatile("s_waitcnt lgkmcnt(0)" ::: "memory"); __builtin_amdgcn_s_barrier(); asm volatile("" ::: "memory"); } while (0)

__device__ __forceinline__ u32 pk(float lo, float hi) { bf2 r = {(__bf16)lo, (__bf16)hi}; return __builtin_bit_cast(u32, r); }
__device__ __forceinline__ float rbf(float v) { return (float)(__bf16)v; }
__device__ __forceinline__ float asf(u32 u) { return __builtin_bit_cast(float, u); }
__device__ __forceinline__ u32 asu(float f) { return __builtin_bit_cast(u32, f); }
__device__ __forceinline__ f4 mfma32(v4u a, v4u b, f4 c) { return __builtin_amdgcn_mfma_f32_16x16x32_bf16(__builtin_bit_cast(bf8, a), __builtin_bit_cast(bf8, b), c, 0, 0, 0); }
__device__ __forceinline__ v2u trd(LAS u8* p) { return __builtin_bit_cast(v2u, __builtin_amdgcn_ds_read_tr16_b64_v4i16((LAS s4*)p)); }
__device__ __forceinline__ v4u trd2(LAS u8* p, u32 second) { v2u a = trd(p), b = trd(p + second); return (v4u){a.x, a.y, b.x, b.y}; }
__device__ __forceinline__ v4u ldsr(LAS u8* p) { return *(LAS v4u*)p; }
__device__ __forceinline__ float rows_sum(float x) {
  auto s = __builtin_amdgcn_permlane16_swap(asu(x), asu(x), false, false); x = asf(s[0]) + asf(s[1]);
  auto u = __builtin_amdgcn_permlane32_swap(asu(x), asu(x), false, false); return asf(u[0]) + asf(u[1]);
}
__device__ __forceinline__ float max2(float a, float b) { return __builtin_amdgcn_fmed3f(a, b, __builtin_inff()); }
__device__ __forceinline__ float rows_max(float x) {
  auto s = __builtin_amdgcn_permlane16_swap(asu(x), asu(x), false, false); x = max2(asf(s[0]), asf(s[1]));
  auto u = __builtin_amdgcn_permlane32_swap(asu(x), asu(x), false, false); return max2(asf(u[0]), asf(u[1]));
}
template <int CTRL> __device__ __forceinline__ float dpp(float x) { return asf(__builtin_amdgcn_update_dpp(0, asu(x), CTRL, 0xf, 0xf, true)); }
__device__ __forceinline__ float row16_sum(float x) { x += dpp<0xB1>(x); x += dpp<0x4E>(x); x += dpp<0x141>(x); x += dpp<0x128>(x); return x; }
__device__ __forceinline__ float row16_max(float x) { x = max2(x, dpp<0xB1>(x)); x = max2(x, dpp<0x4E>(x)); x = max2(x, dpp<0x141>(x)); return max2(x, dpp<0x128>(x)); }
__device__ __forceinline__ u64 gran(u64 tag40, float v) { return (tag40 << 24) | (u64)(((asu(v) + 0x80u) >> 8) & 0xFFFFFFu); }
__device__ __forceinline__ void gchk(u32& badh, u32& badl, u32 lo, u32 hi, u32 tl24, u32 thi) { badh |= hi ^ thi; badl |= lo ^ tl24; }
__device__ __forceinline__ bool gfin(u32 badh, u32 badl) { return (badh | (badl >> 24)) == 0; }
__device__ __forceinline__ u32 max4(v4u v) { return max(max(max(v.x, v.y), v.z), v.w); }

extern "C" __global__ void __launch_bounds__(512, 2)
gated_mix_mla_fused(const u16* __restrict__ x, const u16* __restrict__ ckv, const u16* __restrict__ wq, const u16* __restrict__ wuk,
                    const u16* __restrict__ wuv, const u16* __restrict__ wo, const u16* __restrict__ w1, const u16* __restrict__ w2,
                    const u16* __restrict__ gw, u8* __restrict__ ws, u16* __restrict__ out) {
  __shared__ __attribute__((aligned(1024))) u8 smem[LDS_BYTES];
  LAS u8* S = (LAS u8*)smem;
  const u32 lb = (u32)(size_t)S;
  const int t = threadIdx.x, l = t & 63, w = __builtin_amdgcn_readfirstlane(t >> 6);
  const int bx = blockIdx.x & 7, br = blockIdx.x >> 3;
  const int g = l >> 4, q4 = (l & 15) >> 2, p4 = l & 3;
  STAMP(0);
  u64 kstart;
  asm volatile("s_getpc_b64 %0" : "=s"(kstart));
  kstart &= ~255ull;
#define ROLE_PROLOGUE()                                                                                                             \
  asm volatile("" ::"s"(x), "s"(ckv), "s"(wq), "s"(wuk), "s"(wuv), "s"(wo), "s"(w1), "s"(w2), "s"(gw), "s"(ws), "s"(out));          \
  const __amdgpu_buffer_rsrc_t rs = __builtin_amdgcn_make_buffer_rsrc(ws, 0, W_END, 0x00020000);                                     \
  if (t == 0) dma16c(0u, ws + W_EPOCH, lb + M_EV);                                                                                  \
  CODE_PREFETCH()
#define CODE_PREFETCH() \
  if (w == 1) dma4((u32)br * (PREFETCH_BYTES / 32) + min((u32)l * 64u, PREFETCH_BYTES / 32 - 4u), (const void*)kstart, lb + M_PF);
#define READ_TAG() const u64 tag = *(LAS u64*)(S + M_EV) + 1

  if (__builtin_expect(br < 16, 1)) {
    const int li = bx * 16 + br, h = li >> 2, qq = li & 3;
    ROLE_PROLOGUE();
    if (w == 0) { dma16d(l * 16, x, lb + L_X); if (l < 16) dma16d(1024 + l * 16, x, lb + L_X + 1024); }
    if (w == 1) { dma16d(l * 16, gw, lb + L_GW); if (l < 16) dma16d(1024 + l * 16, gw, lb + L_GW + 1024); }
    if (l < 16) __builtin_amdgcn_raw_buffer_store_b128((v4u){SENT, SENT, SENT, SENT}, rs, W_QA + w * QA_REP + h * 1024 + qq * 256 + l * 16, 0, 16);
    {
      const int rr = 4 * w + g, sw = (rr & 3) | (((rr >> 3) & 1) << 2);
      const u32 vo = (u32)(((160 * qq + rr) * 4096 + h * 128) * 2) + (((l & 15) ^ (sw << 1)) << 4);
      const u8* p = (const u8*)wq;
#pragma unroll 1
      for (int i = 0; i < 5; ++i) { dma16(vo, p, lb + L_WQ + i * 8192 + w * 1024); p += 32 * 8192; }
      WAITVM(4);
      const u32 vb = (u32)(((128 * qq + 16 * w + g) * 4096 + h * 128) * 2), vx = ((l & 15) ^ g) << 4;
      p = (const u8*)wuk;
#pragma unroll 1
      for (int i = 0; i < 4; ++i) { dma16(vb + (vx ^ (i << 6)), p, lb + L_WUK + w * 4096 + i * 1024); p += 4 * 8192; }
    }
    WAITVM(4);
    BAR();
    READ_TAG();
    const u64 tag40 = tag & 0xFFFFFFFFFFull;
    const u32 tl24 = (u32)tag40 << 24, thi = (u32)(tag40 >> 8);
    if (t == 0) __hip_atomic_store((gu64*)(ws + W_TOKL) + li, tag, RLX_AGENT);
    STAMP(1);
    {
      LAS u8* b = S + L_WQ + (8 * g + q4) * 256 + ((w ^ (q4 | ((g & 1) << 2))) << 5) + p4 * 8;
      f4 acc = {0.f, 0.f, 0.f, 0.f};
#pragma unroll
      for (int ks = 0; ks < 5; ++ks) acc = mfma32(ldsr(S + L_X + (160 * qq + 32 * ks + 8 * g) * 2), trd2(b + ks * 8192, 1024), acc);
      if (l < 16) __hip_atomic_store((gu64*)(ws + W_QP) + (h * 4 + qq) * 128 + 16 * w + l, gran(tag40, acc[0]), RLX_AGENT);
    }
    STAMP(2);
    if (w == 1 && li < NRED) {
      float s = 0.f;
#pragma unroll
      for (int i = 0; i < 2; ++i) {
        if (i == 0 || l < 16) {
          const v4u a = ldsr(S + L_X + i * 1024 + l * 16), b = ldsr(S + L_GW + i * 1024 + l * 16);
#pragma unroll
          for (int j = 0; j < 4; ++j) s += rbf(asf(a[j] << 16) * asf(b[j] << 16)) + rbf(asf(a[j] & 0xFFFF0000u) * asf(b[j] & 0xFFFF0000u));
        }
      }
      s = rbf(rows_sum(row16_sum(s)));
      if (l == 0) *(LAS float*)(S + L_GATE) = rbf(1.f / (1.f + __expf(-s)));
    }
    WAITVM(0);
    v4u ua[4];
    {
      LAS u8* b = S + L_WUK + w * 4096 + (l & 15) * 256;
      const u32 c0 = (g ^ (l & 15)) << 4;
#pragma unroll
      for (int ks = 0; ks < 4; ++ks) ua[ks] = ldsr(b + (c0 ^ (ks << 6)));
    }
    if (w == 0) {
      float q0, q1;
      for (u32 spin = 0; spin < SPIN_CAP; ++spin) {
        RELOAD();
        v4u v[4];
#pragma unroll
        for (int p = 0; p < 4; ++p) v[p] = __builtin_amdgcn_raw_buffer_load_b128(rs, W_QP + ((h * 4 + p) * 128 + 2 * l) * 8, 0, 16);
        u32 bh = 0, bl = 0;
#pragma unroll
        for (int p = 0; p < 4; ++p) { gchk(bh, bl, v[p].x, v[p].y, tl24, thi); gchk(bh, bl, v[p].z, v[p].w, tl24, thi); }
        q0 = (asf(v[0].x << 8) + asf(v[1].x << 8)) + (asf(v[2].x << 8) + asf(v[3].x << 8));
        q1 = (asf(v[0].z << 8) + asf(v[1].z << 8)) + (asf(v[2].z << 8) + asf(v[3].z << 8));
        if (__all(gfin(bh, bl))) break;
      }
      *(LAS u32*)(S + L_Q + l * 4) = pk(q0, q1);
    }
    BAR();
    STAMP(3);
    {
      f4 acc = {0.f, 0.f, 0.f, 0.f};
#pragma unroll
      for (int ks = 0; ks < 4; ++ks) acc = mfma32(ua[ks], ldsr(S + L_Q + ks * 64 + g * 16), acc);
      constexpr float SC = 0.08838834764831845f * 1.4426950408889634f;
      if ((l & 15) == 0) *(LAS v2u*)(S + L_QA + w * 32 + g * 8) = (v2u){min(pk(acc[0] * SC, acc[1] * SC), SENT - 1), min(pk(acc[2] * SC, acc[3] * SC), SENT - 1)};
      BAR();
      if (l < 16) __builtin_amdgcn_raw_buffer_store_b128(ldsr(S + L_QA + l * 16), rs, W_QA + w * QA_REP + h * 1024 + qq * 256 + l * 16, 0, 16);
    }
    STAMP(4);
    if (li < NFFN) {
      __builtin_amdgcn_s_sleep(F_DELAY);
      {
        const u32 vo = (u32)((32 * w + (l >> 1)) * (F * 2) + 32 * li + (l & 1) * 16);
        const u8* p = (const u8*)w1;
#pragma unroll 1
        for (int ks = w; ks < 20; ks += 8) { dma16(vo, p, lb + L_W1 + ks * 1024); p += 256 * (F * 2); }
        p = (const u8*)w2 + (size_t)li * 16 * 1280;
#pragma unroll 1
        for (int i = w; i < 20; i += 8) {
          const u32 u = 64 * i + l, row = (u * 52429u) >> 22, pos = u - row * 80;
          dma16(row * 1280 + ((pos ^ ((row & 7) << 1)) << 4), p, lb + L_W2 + i * 1024);
        }
      }
      WAITVM(2);
      {
        f4 acc = {0.f, 0.f, 0.f, 0.f};
        LAS u8* b = S + L_W1 + w * 1024 + (4 * g + q4) * 32 + p4 * 8;
        LAS u8* xp = S + L_X + (32 * w + 4 * g) * 2;
        auto xfrag = [&](int o) { const v2u a = *(LAS v2u*)(xp + o), c = *(LAS v2u*)(xp + o + 32); return (v4u){a.x, a.y, c.x, c.y}; };
        acc = mfma32(xfrag(0), trd2(b, 512), acc);
        acc = mfma32(xfrag(512), trd2(b + 8192, 512), acc);
        if (w < 4) acc = mfma32(xfrag(1024), trd2(b + 16384, 512), acc);
        if (l < 16) *(LAS float*)(S + L_RED + (l * 8 + w) * 4) = acc[0];
      }
      WAITVM(0);
      BAR();
      {
        float hv[4];
#pragma unroll
        for (int j = 0; j < 4; ++j) {
          const f4 a = *(LAS f4*)(S + L_RED + (4 * g + j) * 32), b = *(LAS f4*)(S + L_RED + (4 * g + j) * 32 + 16);
          const float s = rbf(((a[0] + a[1]) + (a[2] + a[3])) + ((b[0] + b[1]) + (b[2] + b[3])));
          hv[j] = s / (1.f + __expf(-s));
        }
        const v2u ha = {pk(hv[0], hv[1]), pk(hv[2], hv[3])};
        const int row = 4 * g + q4;
#pragma unroll
        for (int n = 0; n < 5; ++n) {
          const int T = 5 * w + n;
          const v2u bb = trd(S + L_W2 + row * 1280 + ((T ^ (row & 7)) << 5) + p4 * 8);
          f4 acc = {0.f, 0.f, 0.f, 0.f};
          acc = __builtin_amdgcn_mfma_f32_16x16x16bf16_1k(__builtin_bit_cast(s4, ha), __builtin_bit_cast(s4, bb), acc, 0, 0, 0);
          if (l < 16) __hip_atomic_store((gu64*)(ws + W_PART) + (T * NPART + 64 + li) * 16 + l, gran(tag40, acc[0]), RLX_AGENT);
        }
      }
    }
    STAMP(5);
    if (li < NRED) {
      float sb;
      const int p0 = t >> 4;
      for (u32 spin = 0; spin < SPIN_CAP; ++spin) {
        RELOAD();
        v2u v[4];
#pragma unroll
        for (int i = 0; i < 4; ++i)
          v[i] = __builtin_amdgcn_raw_buffer_load_b64(rs, W_PART + ((li * NPART + min(64 + p0 + 32 * i, NPART - 1)) * 16 + (l & 15)) * 8, 0, 16);
        u32 bh = 0, bl = 0;
#pragma unroll
        for (int i = 0; i < 4; ++i) gchk(bh, bl, v[i].x, v[i].y, tl24, thi);
        sb = (asf(v[0].x << 8) + asf(v[1].x << 8)) + asf(v[2].x << 8);
        if (p0 + 96 < NFFN) sb += asf(v[3].x << 8);
        if (__all(gfin(bh, bl))) break;
      }
      sb = rows_sum(sb);
      if (l < 16) *(LAS float*)(S + L_FIN + (l * 8 + w) * 4) = sb;
      BAR();
      if (w == 0) {
        const float gate = *(LAS float*)(S + L_GATE);
        const int col = 2 * (l & 7) + (l >> 5);
        const f4 b0 = *(LAS f4*)(S + L_FIN + col * 32), b1 = *(LAS f4*)(S + L_FIN + col * 32 + 16);
        const float fb = rbf(rbf(1.f - gate) * rbf(((b0[0] + b0[1]) + (b0[2] + b0[3])) + ((b1[0] + b1[1]) + (b1[2] + b1[3]))));
        float a0, a1;
        for (u32 spin = 0; spin < SPIN_CAP; ++spin) {
          RELOAD();
          v4u v[8];
#pragma unroll
          for (int i = 0; i < 8; ++i) v[i] = __builtin_amdgcn_raw_buffer_load_b128(rs, W_PART + (li * NPART + (l >> 3)) * 128 + (l & 7) * 16 + i * 1024, 0, 16);
          u32 bh = 0, bl = 0;
          a0 = 0.f; a1 = 0.f;
#pragma unroll
          for (int i = 0; i < 8; ++i) {
            gchk(bh, bl, v[i].x, v[i].y, tl24, thi); gchk(bh, bl, v[i].z, v[i].w, tl24, thi);
            a0 += asf(v[i].x << 8); a1 += asf(v[i].z << 8);
          }
          if (__all(gfin(bh, bl))) break;
        }
        STAMP(7);
        auto sw = __builtin_amdgcn_permlane32_swap(asu(a0), asu(a1), false, false);
        float a = asf(sw[0]) + asf(sw[1]);
        auto s2 = __builtin_amdgcn_permlane16_swap(asu(a), asu(a), false, false);
        a = asf(s2[0]) + asf(s2[1]);
        a += dpp<0x128>(a);
        if ((l & 0x18) == 0) out[16 * li + col] = __builtin_bit_cast(u16, (__bf16)(rbf(gate * rbf(a)) + fb));
        if (li == 0 && l == 0) __hip_atomic_store((gu64*)(ws + W_EPOCH), tag, RLX_AGENT);
      }
    }
    STAMP(6);
  } else if (br < 24) {
    const int ai = bx * 8 + (br - 16), chunk = ai >> 1, hg = ai & 1;
    ROLE_PROLOGUE();
    const int oh0 = 16 * hg + (l >> 3);
    const u32 opl0 = W_OP + oh0 * 32768 + ((8 * chunk + w + 9 * oh0) & 255) * 128 + (l & 7) * 16;
    const u32 opl1 = W_OP + (oh0 + 8) * 32768 + ((8 * chunk + w + 9 * (oh0 + 8)) & 255) * 128 + (l & 7) * 16;
    {
      const v4u sent = {SENT, SENT, SENT, SENT};
      __builtin_amdgcn_raw_buffer_store_b128(sent, rs, opl0, 0, 16);
      __builtin_amdgcn_raw_buffer_store_b128(sent, rs, opl1, 0, 16);
      if (t < 32) __builtin_amdgcn_raw_buffer_store_b128(sent, rs, W_ML + ((hg * 4 + (t >> 3)) * 32 + chunk) * 128 + (t & 7) * 16, 0, 16);
    }
    {
      auto fsw = [](int k) { return ((k & 3) << 1) | ((((k >> 2) ^ (k >> 3)) & 1) << 3) | ((k >> 3) & 1); };
      const u32 v0 = (u32)(l ^ fsw(w)) << 4, v1 = (u32)(l ^ fsw(w + 8)) << 4;
      const u8* p = (const u8*)ckv + (size_t)(chunk * 128 + w) * 1024;
#pragma unroll 1
      for (int ii = 0; ii < 8; ++ii) {
        const int i = C_SWAP ? (ii ^ (hg << 2)) : ii;
        p = (const u8*)ckv + (size_t)(chunk * 128 + w + 16 * i) * 1024;
        dma16(v0, p, lb + A_C + (16 * i + w) * 1024);
        dma16(v1, p + 8192, lb + A_C + (16 * i + 8 + w) * 1024);
      }
    }
    WAITVM(16);
    BAR();
    READ_TAG();
    if (t == 0) __hip_atomic_store((gu64*)(ws + W_TOKA) + hg * 32 + chunk, tag, RLX_AGENT);
    STAMP(1);
    WAITVM(0);
    BAR();
    STAMP(2);
    u64 tk = __hip_atomic_load((gu64*)(ws + W_TOKL) + hg * 64 + l, RLX_AGENT);
    const int kf = ((l & 3) << 1) | ((((l >> 2) ^ (l >> 3)) & 1) << 3) | ((l >> 3) & 1);
    v4u ka[16], va[4][4];
    {
      LAS u8* b = S + A_C + (16 * w + (l & 15)) * 1024;
#pragma unroll
      for (int ks = 0; ks < 16; ++ks) ka[ks] = ldsr(b + (ks >> 2) * 256 + ((((ks & 3) * 4 + g) ^ kf) << 4));
    }
    for (u32 spin = 0; spin < SPIN_CAP && !__all(tk == tag); ++spin) tk = __hip_atomic_load((gu64*)(ws + W_TOKL) + hg * 64 + l, RLX_AGENT);
    STAMP(3);
    {
      const int hh = t >> 5, c = t & 31;
      const u32 qo = W_QA + bx * QA_REP + (16 * hg + hh) * 1024 + c * 16;
      RELOAD();
      v4u a = __builtin_amdgcn_raw_buffer_load_b128(rs, qo, 0, 16), b = __builtin_amdgcn_raw_buffer_load_b128(rs, qo + 512, 0, 16);
      RELOAD();
#pragma unroll
      for (int tt = 0; tt < 2; ++tt) {
        const int k15 = 8 * (g & 1) + 4 * tt + q4;
        const int ff = ((k15 & 3) << 1) | ((((k15 >> 2) ^ (k15 >> 3)) & 1) << 3) | ((k15 >> 3) & 1);
        LAS u8* cp = S + A_C + (8 * g + 4 * tt + q4) * 1024 + (w >> 1) * 256 + (p4 & 1) * 8;
        const u32 lo = (u32)((8 * (w & 1) + (p4 >> 1)) ^ ff) << 4;
#pragma unroll
        for (int ks = 0; ks < 4; ++ks)
#pragma unroll
          for (int ct = 0; ct < 4; ++ct) {
            const v2u r = trd(cp + ks * 32768 + (lo ^ (ct << 5)));
            if (tt == 0) { va[ks][ct].x = r.x; va[ks][ct].y = r.y; } else { va[ks][ct].z = r.x; va[ks][ct].w = r.y; }
          }
      }
      for (u32 spin = 0; spin < SPIN_CAP && !__all(max(max4(a), max4(b)) != SENT); ++spin) {
        RELOAD();
        a = __builtin_amdgcn_raw_buffer_load_b128(rs, qo, 0, 16);
        b = __builtin_amdgcn_raw_buffer_load_b128(rs, qo + 512, 0, 16);
      }
      *(LAS v4u*)(S + A_QA + hh * 1024 + ((c ^ hh) << 4)) = a;
      *(LAS v4u*)(S + A_QA + hh * 1024 + 512 + ((c ^ hh) << 4)) = b;
    }
    BAR();
    STAMP(4);
    f4 s0 = {0.f, 0.f, 0.f, 0.f}, s1 = {0.f, 0.f, 0.f, 0.f};
    {
      LAS u8* b = S + A_QA + (l & 15) * 1024;
      v4u qb[16];
#pragma unroll
      for (int ks = 0; ks < 16; ++ks) qb[ks] = ldsr(b + (ks >> 2) * 256 + ((((ks & 3) * 4 + g) ^ (l & 15)) << 4));
      PIN();
#pragma unroll
      for (int ks = 0; ks < 16; ks += 2) { s0 = mfma32(ka[ks], qb[ks], s0); s1 = mfma32(ka[ks + 1], qb[ks + 1], s1); }
    }
    s0 += s1;
    asm volatile("" : "+v"(s0));
    STAMP(7);
    const float wm = rows_max(max2(max2(s0[0], s0[1]), max2(s0[2], s0[3])));
    if (l < 16) *(LAS float*)(S + A_MX + (l * 8 + w) * 4) = wm;
    BAR();
    STAMP(8);
    float mx;
    {
      const f4 a = *(LAS f4*)(S + A_MX + (l & 15) * 32), b = *(LAS f4*)(S + A_MX + (l & 15) * 32 + 16);
      mx = max2(max2(max2(a[0], a[1]), max2(a[2], a[3])), max2(max2(b[0], b[1]), max2(b[2], b[3])));
    }
    {
      const float p0 = __builtin_amdgcn_exp2f(s0[0] - mx), p1 = __builtin_amdgcn_exp2f(s0[1] - mx), p2 = __builtin_amdgcn_exp2f(s0[2] - mx),
                  p3 = __builtin_amdgcn_exp2f(s0[3] - mx);
      *(LAS v2u*)(S + A_P + (l & 15) * 256 + (((2 * w + (g >> 1)) ^ (l & 15)) << 4) + (g & 1) * 8) = (v2u){pk(p0, p1), pk(p2, p3)};
    }
    BAR();
    STAMP(5);
    f4 o[4];
#pragma unroll
    for (int ct = 0; ct < 4; ++ct) o[ct] = (f4){0.f, 0.f, 0.f, 0.f};
    v4u pb[4];
#pragma unroll
    for (int ks = 0; ks < 4; ++ks) pb[ks] = ldsr(S + A_P + (l & 15) * 256 + (((4 * ks + g) ^ (l & 15)) << 4));
    PIN();
#pragma unroll
    for (int ks = 0; ks < 4; ++ks)
#pragma unroll
      for (int ct = 0; ct < 4; ++ct) o[ct] = mfma32(va[ks][ct], pb[ks], o[ct]);
    asm volatile("" : "+v"(o[0]), "+v"(o[1]), "+v"(o[2]), "+v"(o[3]));
    STAMP(9);
    {
      LAS u8* tb = S + A_QA + w * 2048;
#pragma unroll
      for (int ct = 0; ct < 4; ++ct)
        *(LAS v2u*)(tb + (l & 15) * 128 + ((ct * 32 + g * 8) ^ ((l & 7) << 4))) = (v2u){min(pk(o[ct][0], o[ct][1]), SENT - 1), min(pk(o[ct][2], o[ct][3]), SENT - 1)};
      asm volatile("s_waitcnt lgkmcnt(0)" ::: "memory");
      const int hr = l >> 3;
      const v4u r0 = ldsr(tb + hr * 128 + (((l & 7) ^ hr) << 4)), r1 = ldsr(tb + 1024 + hr * 128 + (((l & 7) ^ hr) << 4));
      __builtin_amdgcn_raw_buffer_store_b128(r0, rs, opl0, 0, 16);
      __builtin_amdgcn_raw_buffer_store_b128(r1, rs, opl1, 0, 16);
      if (w == 0) {
        f4 ls = {0.f, 0.f, 0.f, 0.f};
#pragma unroll
        for (int ks = 0; ks < 4; ++ks) ls = mfma32((v4u){0x3F803F80u, 0x3F803F80u, 0x3F803F80u, 0x3F803F80u}, pb[ks], ls);
        if (l < 16) __builtin_amdgcn_raw_buffer_store_b64((v2u){min(asu(mx), SENT - 1), min(asu(ls[0]), SENT - 1)}, rs, W_ML + ((hg * 4 + (l >> 2)) * 32 + chunk) * 128 + (l & 3) * 8, 0, 16);
      }
    }
    STAMP(6);
  } else {
    const int vi = bx * 8 + (br - 24), h = vi >> 1, dh = vi & 1;
    ROLE_PROLOGUE();
    if (t == 0) *(volatile LAS u32*)(S + V_FLAG) = 0;
    __builtin_amdgcn_s_sleep(V_DELAY);
    {
      const int rr = l >> 3;
      const u32 vo = (u32)((64 * w + rr) * 8192 + h * 256 + dh * 128) + (((l & 7) ^ (((rr >> 1) & 1) << 1)) << 4);
      const u8* p = (const u8*)wuv;
#pragma unroll 1
      for (int i = 0; i < 8; ++i) { dma16(vo ^ ((i & 1) << 6), p, lb + V_WUV + w * 8192 + i * 1024); p += 8 * 8192; }
      p = (const u8*)wo + (size_t)(h * 128 + 64 * dh) * 1280;
#pragma unroll 1
      for (int i = 0; i < 10; ++i) {
        const u32 u = 512 * i + 64 * w + l, row = (u * 52429u) >> 22, pos = u - row * 80;
        const u32 sw = (row & 3) | (((row >> 3) & 1) << 2);
        dma16(row * 1280 + ((pos ^ (sw << 1)) << 4), p, lb + V_WO + (8 * i + w) * 1024);
      }
    }
    WAITVM(10);
    v4u b4[2][4], b5[5][2];
    {
      const u32 sw = ((q4 >> 1) & 1) | ((g & 1) << 1);
      LAS u8* b = S + V_WUV + (64 * w + 8 * g + q4) * 128 + p4 * 8;
#pragma unroll
      for (int ks = 0; ks < 2; ++ks)
#pragma unroll
        for (int ct = 0; ct < 4; ++ct) b4[ks][ct] = trd2(b + ks * 4096 + ((ct ^ sw) << 5), 512);
    }
    WAITVM(0);
    BAR();
    READ_TAG();
    const u64 tag40 = tag & 0xFFFFFFFFFFull;
    STAMP(1);
    {
      const u32 sw = q4 | ((g & 1) << 2);
      LAS u8* b = S + V_WO + (8 * g + q4) * 1280 + p4 * 8;
#pragma unroll
      for (int n = 0; n < 5; ++n)
#pragma unroll
        for (int ks = 0; ks < 2; ++ks) b5[n][ks] = trd2(b + ks * 32 * 1280 + (((5 * w + n) ^ sw) << 5), 4 * 1280);
    }
    for (u32 spin = 0; spin < SPIN_CAP; ++spin)
      if (__all(__hip_atomic_load((gu64*)(ws + W_TOKA) + (h >> 4) * 32 + (l & 31), RLX_AGENT) == tag)) break;
    STAMP(2);
    {
      v2u ml;
      if (w == 0)
        for (u32 spin = 0; spin < SPIN_CAP; ++spin) {
          RELOAD();
          ml = __builtin_amdgcn_raw_buffer_load_b64(rs, W_ML + ((h >> 2) * 32 + (l & 31)) * 128 + (h & 3) * 8, 0, 16);
          if (__all(max(ml.x, ml.y) != SENT)) break;
        }
      STAMP(8);
      __builtin_amdgcn_s_barrier();
      const u32 stg = V_WUV + w * 8192;
      const u32 src = ((l & 7) ^ ((l >> 4) & 3)) * 16;
      v4u v[2][2];
      for (u32 spin = 0; spin < SPIN_CAP; ++spin) {
#pragma unroll
        for (int i = 0; i < 4; ++i)
          dma16c(h * 32768 + ((8 * (8 * i + (l >> 3)) + w + 9 * h) & 255) * 128 + (src ^ ((i & 1) << 6)), ws + W_OP, lb + stg + i * 1024);
        if (spin == 0 && w == 0) {
          const float mm = asf(ml.x), gm = rows_max(row16_max(mm)), wt = __builtin_amdgcn_exp2f(mm - gm);
          const float lt = rows_sum(row16_sum(wt * asf(ml.y)));
          if (l < 32) *(volatile LAS float*)(S + V_O + l * 4) = wt * (2.f * __builtin_amdgcn_rcpf(lt));
          asm volatile("s_waitcnt lgkmcnt(0)" ::: "memory");
          *(volatile LAS u32*)(S + V_FLAG) = 1;
        }
        WAITVM(0);
#pragma unroll
        for (int rt = 0; rt < 2; ++rt)
#pragma unroll
          for (int ks = 0; ks < 2; ++ks) v[rt][ks] = ldsr(S + stg + (16 * rt + (l & 15)) * 128 + (((4 * ks + g) ^ (((l & 15) >> 1) & 7)) << 4));
        if (__all(max(max(max4(v[0][0]), max4(v[0][1])), max(max4(v[1][0]), max4(v[1][1]))) != SENT)) break;
      }
      STAMP(3);
      f4 y[2][4];
#pragma unroll
      for (int rt = 0; rt < 2; ++rt)
#pragma unroll
        for (int ct = 0; ct < 4; ++ct) {
          y[rt][ct] = mfma32(v[rt][0], b4[0][ct], (f4){0.f, 0.f, 0.f, 0.f});
          y[rt][ct] = mfma32(v[rt][1], b4[1][ct], y[rt][ct]);
        }
      for (u32 spin = 0; spin < SPIN_CAP && *(volatile LAS u32*)(S + V_FLAG) != 1; ++spin) { }
      const f4 wt0 = *(volatile LAS f4*)(S + V_O + g * 16), wt1 = *(volatile LAS f4*)(S + V_O + 64 + g * 16);
      const float wt[2][4] = {{wt0[0], wt0[1], wt0[2], wt0[3]}, {wt1[0], wt1[1], wt1[2], wt1[3]}};
      float cs[4];
#pragma unroll
      for (int ct = 0; ct < 4; ++ct) {
        cs[ct] = wt[0][0] * y[0][ct][0];
#pragma unroll
        for (int i = 1; i < 8; ++i) cs[ct] += wt[i >> 2][i & 3] * y[i >> 2][ct][i & 3];
      }
      auto e0 = __builtin_amdgcn_permlane32_swap(asu(cs[0]), asu(cs[2]), false, false);
      auto e1 = __builtin_amdgcn_permlane32_swap(asu(cs[1]), asu(cs[3]), false, false);
      auto e2 = __builtin_amdgcn_permlane16_swap(asu(asf(e0[0]) + asf(e0[1])), asu(asf(e1[0]) + asf(e1[1])), false, false);
      const float cx = asf(e2[0]) + asf(e2[1]);
      const __bf16 hi = (__bf16)cx, lo = (__bf16)(cx - (float)hi);
      *(LAS __bf16*)(S + V_CP + w * 144 + l * 2) = hi;
      *(LAS __bf16*)(S + V_CP + (8 + w) * 144 + l * 2) = lo;
      STAMP(9);
    }
    BAR();
    STAMP(4);
    {
      const v4u a0 = ldsr(S + V_CP + (l & 15) * 144 + g * 16), a1 = ldsr(S + V_CP + (l & 15) * 144 + 64 + g * 16);
      float r[5];
#pragma unroll
      for (int n = 0; n < 5; ++n) {
        f4 acc = mfma32(a0, b5[n][0], (f4){0.f, 0.f, 0.f, 0.f});
        acc = mfma32(a1, b5[n][1], acc);
        r[n] = (acc[0] + acc[1]) + (acc[2] + acc[3]);
      }
      auto e0 = __builtin_amdgcn_permlane32_swap(asu(r[0]), asu(r[2]), false, false);
      auto e1 = __builtin_amdgcn_permlane32_swap(asu(r[1]), asu(r[3]), false, false);
      auto e2 = __builtin_amdgcn_permlane16_swap(asu(asf(e0[0]) + asf(e0[1])), asu(asf(e1[0]) + asf(e1[1])), false, false);
      const float r4 = rows_sum(r[4]);
      __hip_atomic_store((gu64*)(ws + W_PART) + ((5 * w + g) * NPART + vi) * 16 + (l & 15), gran(tag40, asf(e2[0]) + asf(e2[1])), RLX_AGENT);
      if (l < 16) __hip_atomic_store((gu64*)(ws + W_PART) + ((5 * w + 4) * NPART + vi) * 16 + l, gran(tag40, r4), RLX_AGENT);
    }
    STAMP(5);
  }
}

extern "C" void kernel_launch(void* const* d_in, const int* in_sizes, int n_in, void* d_out, int out_size, void* d_ws, size_t ws_size,
                              hipStream_t stream) {
  gated_mix_mla_fused<<<256, 512, 0, stream>>>((const u16*)d_in[0], (const u16*)d_in[1], (const u16*)d_in[2], (const u16*)d_in[3],
                                               (const u16*)d_in[4], (const u16*)d_in[5], (const u16*)d_in[7], (const u16*)d_in[8],
                                               (const u16*)d_in[9], (u8*)d_ws + WS_BASE, (u16*)d_out);
}
```

```cpp
#include <hip/hip_runtime.h>
#include <hip/hip_bf16.h>


typedef unsigned short u16;
typedef unsigned u32;
typedef unsigned long long u64;
typedef unsigned char u8;
typedef u32 v4u __attribute__((ext_vector_type(4)));
typedef u32 v2u __attribute__((ext_vector_type(2)));
typedef float f4 __attribute__((ext_vector_type(4)));
typedef __bf16 bf8 __attribute__((ext_vector_type(8)));
typedef __bf16 bf2 __attribute__((ext_vector_type(2)));
typedef short s4 __attribute__((ext_vector_type(4)));
#define LAS __attribute__((address_space(3)))

constexpr int H = 32, SK = 4096, D = 128, M = 640, DC = 512, F = 1728;
constexpr int NFFN = 108, NFS = 27, NRED = 40, NPART = 64 + NFS;

constexpr u32 W_EPOCH = 0;
constexpr u32 W_TOKL = 4096;
constexpr u32 W_TOKA = 8192;
constexpr u32 W_QP = 16384;
constexpr u32 QA_REP = 32768 + 256;
constexpr u32 W_QA = W_QP + 32 * 4 * 128 * 8;
constexpr u32 W_ML = W_QA + 8 * QA_REP;
constexpr u32 W_OP = W_ML + 8 * 32 * 128;
constexpr u32 W_HP = W_OP + 32 * 32768;
constexpr u32 W_PART = W_HP + NFS * 4 * 64 * 8;
constexpr u32 W_END = W_PART + NRED * NPART * 16 * 8;

constexpr u32 L_WQ = 0, L_WUK = 40960, L_W1 = 73728, L_W2 = 94208, L_Q = 114688, L_HV = 115200, L_QA = 116224, L_FIN = 116480;
constexpr u32 L_GATE = 117504, L_X = 117760, L_GW = 119808;
constexpr u32 L_XW = 121856, L_H1 = L_W1;
constexpr u32 A_C = 0, A_QA = 131072, A_P = 147456, A_MX = 151552, M_TK = A_P;
constexpr u32 V_TK = 150016;
constexpr u32 V_WUV = 0, V_WO = 65536, V_O = 147456, V_FLAG = 147584, V_CP = 147712;
constexpr u32 M_EV = 152320, M_PF = 153344;
constexpr u32 LDS_BYTES = 153600;

constexpr int U_DELAY = 27, U_DELAY_SLOW = 18;
constexpr int H1_GAP = 25;
constexpr int F_DELAY = 34;
constexpr int V_DELAY = 78, V_DELAY_SLOW = 33;
constexpr int G_DELAY = 7, R_DELAY = 7;
constexpr u32 PREFETCH_BYTES = 12288;
constexpr u32 SPIN_CAP = 100000;
constexpr u32 SENT = 0xFFFFFFFFu;

#define DMA_ASM(OP, POL) asm volatile("s_mov_b32 m0, %2\n\ts_nop 0\n\t" OP " %0, %1" POL ::"v"(voff), "s"(sbase), "s"(ldsaddr) : "memory")
__device__ __forceinline__ void dma16(u32 voff, const void* sbase, u32 ldsaddr) { DMA_ASM("global_load_lds_dwordx4", " nt"); }
__device__ __forceinline__ void dma16d(u32 voff, const void* sbase, u32 ldsaddr) { DMA_ASM("global_load_lds_dwordx4", ""); }
__device__ __forceinline__ void dma16c(u32 voff, const void* sbase, u32 ldsaddr) { DMA_ASM("global_load_lds_dwordx4", " sc1"); }
__device__ __forceinline__ void dma4(u32 voff, const void* sbase, u32 ldsaddr) { DMA_ASM("global_load_lds_dword", ""); }
#define WAITVM(n) asm volatile("s_waitcnt vmcnt(" #n ")" ::: "memory")
#define WAITLDS() asm volatile("s_waitcnt lgkmcnt(0)" ::: "memory")
#define PIN() __builtin_amdgcn_sched_barrier(0)
#define RELOAD() asm volatile("" ::: "memory")
#define BAR() do { WAITLDS(); __builtin_amdgcn_s_barrier(); asm volatile("" ::: "memory"); } while (0)
#define LD16(off) __builtin_amdgcn_raw_buffer_load_b128(rs, (off), 0, 16)
#define LD8(off) __builtin_amdgcn_raw_buffer_load_b64(rs, (off), 0, 16)
#define ST16(v, off) __builtin_amdgcn_raw_buffer_store_b128((v), rs, (off), 0, 16)
#define ST8(v, off) __builtin_amdgcn_raw_buffer_store_b64((v), rs, (off), 0, 16)

__device__ __forceinline__ u32 pk(float lo, float hi) { bf2 r = {(__bf16)lo, (__bf16)hi}; return __builtin_bit_cast(u32, r); }
__device__ __forceinline__ float rbf(float v) { return (float)(__bf16)v; }
__device__ __forceinline__ float asf(u32 u) { return __builtin_bit_cast(float, u); }
__device__ __forceinline__ u32 asu(float f) { return __builtin_bit_cast(u32, f); }
__device__ __forceinline__ f4 mfma32(v4u a, v4u b, f4 c) { return __builtin_amdgcn_mfma_f32_16x16x32_bf16(__builtin_bit_cast(bf8, a), __builtin_bit_cast(bf8, b), c, 0, 0, 0); }
__device__ __forceinline__ v2u trd(LAS u8* p) { return __builtin_bit_cast(v2u, __builtin_amdgcn_ds_read_tr16_b64_v4i16((LAS s4*)p)); }
__device__ __forceinline__ v4u trd2(LAS u8* p, u32 second) { v2u a = trd(p), b = trd(p + second); return (v4u){a.x, a.y, b.x, b.y}; }
__device__ __forceinline__ v4u ldsr(LAS u8* p) { return *(LAS v4u*)p; }
__device__ __forceinline__ float rows_sum(float x) {
  auto s = __builtin_amdgcn_permlane16_swap(asu(x), asu(x), false, false); x = asf(s[0]) + asf(s[1]);
  auto u = __builtin_amdgcn_permlane32_swap(asu(x), asu(x), false, false); return asf(u[0]) + asf(u[1]);
}
__device__ __forceinline__ float rows_max(float x) {
  auto s = __builtin_amdgcn_permlane16_swap(asu(x), asu(x), false, false); x = fmaxf(asf(s[0]), asf(s[1]));
  auto u = __builtin_amdgcn_permlane32_swap(asu(x), asu(x), false, false); return fmaxf(asf(u[0]), asf(u[1]));
}
__device__ __forceinline__ float rows_sum_scatter(float a, float b, float c, float d) {
  auto e0 = __builtin_amdgcn_permlane32_swap(asu(a), asu(c), false, false);
  auto e1 = __builtin_amdgcn_permlane32_swap(asu(b), asu(d), false, false);
  auto e2 = __builtin_amdgcn_permlane16_swap(asu(asf(e0[0]) + asf(e0[1])), asu(asf(e1[0]) + asf(e1[1])), false, false);
  return asf(e2[0]) + asf(e2[1]);
}
template <int CTRL> __device__ __forceinline__ float dpp(float x) { return asf(__builtin_amdgcn_update_dpp(0, asu(x), CTRL, 0xf, 0xf, true)); }
__device__ __forceinline__ float row16_sum(float x) { x += dpp<0xB1>(x); x += dpp<0x4E>(x); x += dpp<0x141>(x); x += dpp<0x128>(x); return x; }
__device__ __forceinline__ float row16_max(float x) { x = fmaxf(x, dpp<0xB1>(x)); x = fmaxf(x, dpp<0x4E>(x)); x = fmaxf(x, dpp<0x141>(x)); return fmaxf(x, dpp<0x128>(x)); }
__device__ __forceinline__ v2u gran(u32 tl24, u32 thi, float v) { return (v2u){((asu(v) + 0x80u) >> 8) | tl24, thi}; }
__device__ __forceinline__ void gchk(u32& badh, u32& badl, u32 lo, u32 hi, u32 tl24, u32 thi) { badh |= hi ^ thi; badl |= lo ^ tl24; }
__device__ __forceinline__ bool gfin(u32 badh, u32 badl) { return (badh | (badl >> 24)) == 0; }
__device__ __forceinline__ u32 max4(v4u v) { return max(max(max(v.x, v.y), v.z), v.w); }

extern "C" __global__ void __launch_bounds__(512, 2)
gated_mix_mla_fused(const u16* __restrict__ x, const u16* __restrict__ ckv, const u16* __restrict__ wq, const u16* __restrict__ wuk,
                    const u16* __restrict__ wuv, const u16* __restrict__ wo, const u16* __restrict__ w1, const u16* __restrict__ w2,
                    const u16* __restrict__ gw, u8* __restrict__ ws, u16* __restrict__ out) {
  __shared__ __attribute__((aligned(1024))) u8 smem[LDS_BYTES];
  LAS u8* S = (LAS u8*)smem;
  const u32 lb = (u32)(size_t)S;
  const int t = threadIdx.x, l = t & 63, w = __builtin_amdgcn_readfirstlane(t >> 6);
  const int bx = blockIdx.x & 7, br = blockIdx.x >> 3;
  const int g = l >> 4, q4 = (l & 15) >> 2, p4 = l & 3;
  u64 kstart;
  asm volatile("s_getpc_b64 %0" : "=s"(kstart));
  kstart &= ~255ull;
#define ROLE_ARGS()                                                                                                        \
  u8* wsr = ws;                                                                                                            \
  asm volatile("" : "+s"(wsr) : "s"(x), "s"(ckv), "s"(wq), "s"(wuk), "s"(wuv), "s"(wo), "s"(w1), "s"(w2), "s"(gw), "s"(out)); \
  const __amdgpu_buffer_rsrc_t rs = __builtin_amdgcn_make_buffer_rsrc(wsr, 0, W_END, 0x00020000)
#define ROLE_SMALL_LOADS()                                                                                                 \
  if (w == 0) dma16c(l * 16, ws + W_EPOCH, lb + M_EV);                                                                     \
  if (w == 1) dma4((u32)br * (PREFETCH_BYTES / 32) + min((u32)l * 64u, PREFETCH_BYTES / 32 - 4u), (const void*)kstart, lb + M_PF)
#define READ_TAG()                                                                                                         \
  const u64 tag = *(LAS u64*)(S + M_EV) + 1;                                                                               \
  const u32 tl24 = (u32)tag << 24, thi = (u32)(tag >> 8)

  if (__builtin_expect(br < 16, 1)) {
    const int li = bx * 16 + br, h = li >> 2, qq = li & 3;
    const int ri = br < 5 ? bx * 5 + br : NRED;
    const int rr = 4 * w + g;
    const u32 wqo = (u32)(((160 * qq + rr) * 4096 + h * 128) * 2) + (((l & 15) ^ (((rr & 3) | (((rr >> 3) & 1) << 2)) << 1)) << 4);
    const u32 ukb = (u32)(((128 * qq + 16 * w + g) * 4096 + h * 128) * 2), ukx = ((l & 15) ^ g) << 4;
    const u32 qao = W_QA + w * QA_REP + h * 1024 + qq * 256 + l * 16;
    ROLE_ARGS();
    {
      const u8* p = (const u8*)wq;
#pragma unroll 1
      for (int i = 0; i < 5; ++i) { dma16(wqo, p, lb + L_WQ + i * 8192 + w * 1024); p += 32 * 8192; }
    }
    dma16d(min((u32)(320 * qq) + l * 16, 1264u), x, lb + L_XW + w * 1024);
    ROLE_SMALL_LOADS();
    if ((w & 6) == 2) dma16d(min((u32)(w & 1) * 1024 + l * 16, 1264u), x, lb + L_X + (w & 1) * 1024);
    if ((w & 6) == 4) dma16d(min((u32)(w & 1) * 1024 + l * 16, 1264u), gw, lb + L_GW + (w & 1) * 1024);
    if (l < 16) ST16(((v4u){SENT, SENT, SENT, SENT}), qao);
    if ((((u32)(size_t)wuk + h * 256) >> 8 & 3) == 1) __builtin_amdgcn_s_sleep(U_DELAY_SLOW);
    else __builtin_amdgcn_s_sleep(U_DELAY);
    {
      const u8* p = (const u8*)wuk;
#pragma unroll 1
      for (int i = 0; i < 4; ++i) { dma16(ukb + (ukx ^ (i << 6)), p, lb + L_WUK + w * 4096 + i * 1024); p += 4 * 8192; }
    }
    WAITVM(4);
    v4u xa[5];
#pragma unroll
    for (int ks = 0; ks < 5; ++ks) xa[ks] = ldsr(S + L_XW + w * 1024 + (32 * ks + 8 * g) * 2);
    u32 wqa = lb + L_WQ + (8 * g + q4) * 256 + ((w ^ (q4 | ((g & 1) << 2))) << 5) + p4 * 8, qpo = W_QP + ((h * 4 + qq) * 128 + 16 * w + l) * 8;
    asm volatile("" : "+v"(wqa), "+v"(qpo));
    BAR();
    u32 tl24, thi;
    u64 tag;
    {
      LAS u8* b = (LAS u8*)(size_t)wqa;
      v4u wb[5];
#pragma unroll
      for (int ks = 0; ks < 5; ++ks) wb[ks] = trd2(b + ks * 8192, 1024);
      tag = *(LAS u64*)(S + M_EV) + 1;
      PIN();
      f4 acc = {0.f, 0.f, 0.f, 0.f};
#pragma unroll
      for (int ks = 0; ks < 5; ++ks) acc = mfma32(xa[ks], wb[ks], acc);
      tl24 = (u32)tag << 24; thi = (u32)(tag >> 8);
      if (l < 16) ST8(gran(tl24, thi, acc[0]), qpo);
    }
    if (t == 0) ST8(((v2u){(u32)tag, (u32)(tag >> 32)}), W_TOKL + li * 8);
    if (w == 1 && ri < NRED) {
      float s = 0.f;
#pragma unroll
      for (int i = 0; i < 2; ++i) {
        if (i == 0 || l < 16) {
          const v4u a = ldsr(S + L_X + i * 1024 + l * 16), b = ldsr(S + L_GW + i * 1024 + l * 16);
#pragma unroll
          for (int j = 0; j < 4; ++j) s += rbf(asf(a[j] << 16) * asf(b[j] << 16)) + rbf(asf(a[j] & 0xFFFF0000u) * asf(b[j] & 0xFFFF0000u));
        }
      }
      s = rbf(rows_sum(row16_sum(s)));
      if (l == 0) *(LAS float*)(S + L_GATE) = rbf(1.f / (1.f + __expf(-s)));
    }
    if (w == 0) {
      __builtin_amdgcn_s_sleep(H1_GAP);
#pragma unroll
      for (int p = 0; p < 4; ++p) dma16c(p * 1024 + l * 16, ws + W_QP + h * 4096, lb + L_H1 + p * 1024);
    }
    WAITVM(0);
    v4u ua[4];
    {
      LAS u8* b = S + L_WUK + w * 4096 + (l & 15) * 256;
      const u32 c0 = (g ^ (l & 15)) << 4;
#pragma unroll
      for (int ks = 0; ks < 4; ++ks) ua[ks] = ldsr(b + (c0 ^ (ks << 6)));
    }
    if (w == 0) {
      float q0, q1;
      v4u v[4];
#pragma unroll
      for (int p = 0; p < 4; ++p) v[p] = ldsr(S + L_H1 + p * 1024 + l * 16);
      _Pragma("nounroll") for (u32 spin = 0; spin < SPIN_CAP; ++spin) {
        u32 bh = 0, bl = 0;
#pragma unroll
        for (int p = 0; p < 4; ++p) { gchk(bh, bl, v[p].x, v[p].y, tl24, thi); gchk(bh, bl, v[p].z, v[p].w, tl24, thi); }
        q0 = (asf(v[0].x << 8) + asf(v[1].x << 8)) + (asf(v[2].x << 8) + asf(v[3].x << 8));
        q1 = (asf(v[0].z << 8) + asf(v[1].z << 8)) + (asf(v[2].z << 8) + asf(v[3].z << 8));
        if (__all(gfin(bh, bl))) break;
        RELOAD();
#pragma unroll
        for (int p = 0; p < 4; ++p) v[p] = LD16(W_QP + ((h * 4 + p) * 128 + 2 * l) * 8);
      }
      *(LAS u32*)(S + L_Q + l * 4) = pk(q0, q1);
    }
    BAR();
    {
      v4u qb[4];
#pragma unroll
      for (int ks = 0; ks < 4; ++ks) qb[ks] = ldsr(S + L_Q + ks * 64 + g * 16);
      PIN();
      f4 acc = {0.f, 0.f, 0.f, 0.f};
#pragma unroll
      for (int ks = 0; ks < 4; ++ks) acc = mfma32(ua[ks], qb[ks], acc);
      constexpr float SC = 0.08838834764831845f * 1.4426950408889634f;
      if ((l & 15) == 0) *(LAS v2u*)(S + L_QA + w * 32 + g * 8) = (v2u){min(pk(acc[0] * SC, acc[1] * SC), SENT - 1), min(pk(acc[2] * SC, acc[3] * SC), SENT - 1)};
      BAR();
      if (l < 16) ST16(ldsr(S + L_QA + l * 16), qao);
    }
    if (li < NFFN) {
      const int fs = li >> 2, fq = li & 3;
      __builtin_amdgcn_s_sleep(F_DELAY);
      {
        const int r8 = l >> 3;
        const u32 vo = (u32)((160 * fq + 8 * w + r8) * (F * 2) + 128 * fs) + (((l & 7) ^ (((r8 >> 1) & 1) << 1)) << 4);
        const u8* p = (const u8*)w1;
#pragma unroll 1
        for (int i = w; i < 20; i += 8) { dma16(vo ^ ((i & 1) << 6), p, lb + L_W1 + i * 1024); p += 64 * (F * 2); }
        p = (const u8*)w2 + (size_t)fs * 64 * 1280 + fq * 320;
#pragma unroll 1
        for (int i = w; i < 20; i += 8) {
          const u32 u = 64 * i + l, row = (u * 52429u) >> 20, pos = u - row * 20;
          dma16(row * 1280 + ((pos ^ ((row >> 2) & 2)) << 4), p, lb + L_W2 + i * 1024);
        }
      }
      WAITVM(2);
      BAR();
      if (w < 4) {
        const u32 sw = ((q4 >> 1) & 1) | ((g & 1) << 1);
        LAS u8* b = S + L_W1 + (8 * g + q4) * 128 + ((w ^ sw) << 5) + p4 * 8;
        v4u xa[5], wb[5];
#pragma unroll
        for (int ks = 0; ks < 5; ++ks) { xa[ks] = ldsr(S + L_X + (160 * fq + 32 * ks + 8 * g) * 2); wb[ks] = trd2(b + ks * 4096, 512); }
        PIN();
        f4 acc = {0.f, 0.f, 0.f, 0.f};
#pragma unroll
        for (int ks = 0; ks < 5; ++ks) acc = mfma32(xa[ks], wb[ks], acc);
        if (l < 16) ST8(gran(tl24, thi, acc[0]), W_HP + ((fs * 4 + fq) * 64 + 16 * w + l) * 8);
      }
      if (w == 0) {
        float hv;
        _Pragma("nounroll") for (u32 spin = 0; spin < SPIN_CAP; ++spin) {
          RELOAD();
          v2u v[4];
#pragma unroll
          for (int p = 0; p < 4; ++p) v[p] = LD8(W_HP + ((fs * 4 + p) * 64 + l) * 8);
          u32 bh = 0, bl = 0;
#pragma unroll
          for (int p = 0; p < 4; ++p) gchk(bh, bl, v[p].x, v[p].y, tl24, thi);
          hv = rbf((asf(v[0].x << 8) + asf(v[1].x << 8)) + (asf(v[2].x << 8) + asf(v[3].x << 8)));
          if (__all(gfin(bh, bl))) break;
        }
        *(LAS __bf16*)(S + L_HV + l * 2) = (__bf16)(hv / (1.f + __expf(-hv)));
      }
      WAITVM(0);
      BAR();
      {
        const v4u a0 = ldsr(S + L_HV + g * 16), a1 = ldsr(S + L_HV + 64 + g * 16);
        LAS u8* b = S + L_W2 + (8 * g + q4) * 320 + p4 * 8;
#pragma unroll 1
        for (int T = w; T < 10; T += 8) {
          const u32 u = (u32)(T ^ (g & 1)) << 5;
          const f4 acc = mfma32(a1, trd2(b + 32 * 320 + u, 4 * 320), mfma32(a0, trd2(b + u, 4 * 320), (f4){0.f, 0.f, 0.f, 0.f}));
          if (l < 16) ST8(gran(tl24, thi, acc[0]), W_PART + (((10 * fq + T) * NPART + 64 + fs) * 16 + l) * 8);
        }
      }
    }
    if (ri < NRED) {
      float sb;
      const int p0 = t >> 4;
      __builtin_amdgcn_s_sleep(R_DELAY);
      _Pragma("nounroll") for (u32 spin = 0; spin < SPIN_CAP; ++spin) {
        RELOAD();
        const v2u v = LD8(W_PART + ((ri * NPART + 64 + min(p0, NFS - 1)) * 16 + (l & 15)) * 8);
        u32 bh = 0, bl = 0;
        gchk(bh, bl, v.x, v.y, tl24, thi);
        sb = p0 < NFS ? asf(v.x << 8) : 0.f;
        if (__all(gfin(bh, bl))) break;
      }
      sb = rows_sum(sb);
      if (l < 16) *(LAS float*)(S + L_FIN + (l * 8 + w) * 4) = sb;
      {
        v4u v;
        _Pragma("nounroll") for (u32 spin = 0; spin < SPIN_CAP; ++spin) {
          RELOAD();
          v = LD16(W_PART + (ri * NPART + 8 * w + (l >> 3)) * 128 + (l & 7) * 16);
          u32 bh = 0, bl = 0;
          gchk(bh, bl, v.x, v.y, tl24, thi); gchk(bh, bl, v.z, v.w, tl24, thi);
          if (__all(gfin(bh, bl))) break;
        }
        auto sw = __builtin_amdgcn_permlane32_swap(v.x << 8, v.z << 8, false, false);
        float a = asf(sw[0]) + asf(sw[1]);
        auto s2 = __builtin_amdgcn_permlane16_swap(asu(a), asu(a), false, false);
        a = asf(s2[0]) + asf(s2[1]);
        a += dpp<0x128>(a);
        if ((l & 0x18) == 0) *(LAS float*)(S + L_FIN + 512 + ((2 * (l & 7) + (l >> 5)) * 8 + w) * 4) = a;
      }
      BAR();
      if (t < 16) {
        const float gate = *(LAS float*)(S + L_GATE);
        const f4 a0 = *(LAS f4*)(S + L_FIN + 512 + l * 32), a1 = *(LAS f4*)(S + L_FIN + 512 + l * 32 + 16);
        const f4 b0 = *(LAS f4*)(S + L_FIN + l * 32), b1 = *(LAS f4*)(S + L_FIN + l * 32 + 16);
        const float a = rbf(((a0[0] + a0[1]) + (a0[2] + a0[3])) + ((a1[0] + a1[1]) + (a1[2] + a1[3])));
        const float b = rbf(((b0[0] + b0[1]) + (b0[2] + b0[3])) + ((b1[0] + b1[1]) + (b1[2] + b1[3])));
        out[16 * ri + l] = __builtin_bit_cast(u16, (__bf16)(rbf(gate * a) + rbf(rbf(1.f - gate) * b)));
        if (ri == 0 && l == 0) ST8(((v2u){(u32)tag, (u32)(tag >> 32)}), W_EPOCH);
      }
    }
  } else if (br < 24) {
    const int ai = bx * 8 + (br - 16), chunk = ai >> 1, hg = ai & 1;
    auto fsw = [](int k) { return ((k & 3) << 1) | ((((k >> 2) ^ (k >> 3)) & 1) << 3) | ((k >> 3) & 1); };
    const u32 v0 = (u32)(l ^ fsw(w)) << 4, v1 = (u32)(l ^ fsw(w + 8)) << 4;
    const int oh0 = 16 * hg + (l >> 3);
    const u32 opl0 = W_OP + oh0 * 32768 + ((8 * chunk + w + 9 * oh0) & 255) * 128 + (l & 7) * 16;
    const u32 opl1 = W_OP + (oh0 + 8) * 32768 + ((8 * chunk + w + 9 * (oh0 + 8)) & 255) * 128 + (l & 7) * 16;
    const u32 qo = W_QA + bx * QA_REP + (16 * hg + (t >> 5)) * 1024 + (t & 31) * 16;
    ROLE_ARGS();
    ROLE_SMALL_LOADS();
    {
      const v4u sent = {SENT, SENT, SENT, SENT};
      ST16(sent, opl0);
      ST16(sent, opl1);
      if (t < 32) ST16(sent, W_ML + ((hg * 4 + (t >> 3)) * 32 + chunk) * 128 + (t & 7) * 16);
    }
#pragma unroll 1
    for (int ii = 0; ii < 8; ++ii) {
      const int i = ((ii & 2) << 1 | (ii >> 2) << 1 | (ii & 1)) ^ (hg << 2);
      const u8* p = (const u8*)ckv + (size_t)(chunk * 128 + w + 16 * i) * 1024;
      dma16(v0, p, lb + A_C + (16 * i + w) * 1024);
      dma16(v1, p + 8192, lb + A_C + (16 * i + 8 + w) * 1024);
    }
    if (w == 7) dma16c((hg * 64 + (l & 31) * 2) * 8, ws + W_TOKL, lb + M_TK);
    WAITVM(0);
    BAR();
    READ_TAG();
    {
      v2u tk = *(LAS v2u*)(S + M_TK + l * 8);
      if (!__all(tk.x == (u32)tag && tk.y == (u32)(tag >> 32)))
        _Pragma("nounroll") for (u32 spin = 0; spin < SPIN_CAP; ++spin) {
          RELOAD();
          tk = LD8(W_TOKL + (hg * 64 + l) * 8);
          if (__all(tk.x == (u32)tag && tk.y == (u32)(tag >> 32))) break;
        }
    }
    v4u ka[16], va[4][4];
    {
      const int hh = t >> 5, c = t & 31;
      v4u a = LD16(qo), b = LD16(qo + 512);
      RELOAD();
      if (t == 0) ST8(((v2u){(u32)tag, (u32)(tag >> 32)}), W_TOKA + (hg * 32 + chunk) * 8);
      LAS u8* kb = S + A_C + (16 * w + (l & 15)) * 1024;
      const int kf = fsw(l);
#pragma unroll
      for (int ks = 0; ks < 16; ++ks) ka[ks] = ldsr(kb + (ks >> 2) * 256 + ((((ks & 3) * 4 + g) ^ kf) << 4));
    {
      const int f0 = fsw(8 * (g & 1) + q4), f1 = fsw(8 * (g & 1) + 4 + q4);
      LAS u8* cp = S + A_C + (8 * g + q4) * 1024 + (w >> 1) * 256 + (p4 & 1) * 8;
      const u32 lo0 = (u32)((8 * (w & 1) + (p4 >> 1)) ^ f0) << 4, lo1 = (u32)((8 * (w & 1) + (p4 >> 1)) ^ f1) << 4;
#pragma unroll
      for (int ks = 0; ks < 4; ++ks)
#pragma unroll
        for (int ct = 0; ct < 4; ++ct) {
          const v2u r0 = trd(cp + ks * 32768 + (lo0 ^ (ct << 5))), r1 = trd(cp + 4096 + ks * 32768 + (lo1 ^ (ct << 5)));
          va[ks][ct] = (v4u){r0.x, r0.y, r1.x, r1.y};
        }
    }
      _Pragma("nounroll") for (u32 spin = 0; spin < SPIN_CAP && !__all(max(max4(a), max4(b)) != SENT); ++spin) { RELOAD(); a = LD16(qo); b = LD16(qo + 512); }
      *(LAS v4u*)(S + A_QA + hh * 1024 + ((c ^ hh) << 4)) = a;
      *(LAS v4u*)(S + A_QA + hh * 1024 + 512 + ((c ^ hh) << 4)) = b;
    }
    BAR();
    f4 s0 = {0.f, 0.f, 0.f, 0.f}, s1 = {0.f, 0.f, 0.f, 0.f};
    {
      LAS u8* b = S + A_QA + (l & 15) * 1024;
      v4u qb[16];
#pragma unroll
      for (int ks = 0; ks < 16; ++ks) qb[ks] = ldsr(b + (ks >> 2) * 256 + ((((ks & 3) * 4 + g) ^ (l & 15)) << 4));
      PIN();
#pragma unroll
      for (int ks = 0; ks < 16; ks += 2) { s0 = mfma32(ka[ks], qb[ks], s0); s1 = mfma32(ka[ks + 1], qb[ks + 1], s1); }
    }
    s0 += s1;
    const float wm = rows_max(fmaxf(fmaxf(s0[0], s0[1]), fmaxf(s0[2], s0[3])));
    if (l < 16) *(LAS float*)(S + A_MX + (l * 8 + w) * 4) = wm;
    BAR();
    float mx;
    {
      const f4 a = *(LAS f4*)(S + A_MX + (l & 15) * 32), b = *(LAS f4*)(S + A_MX + (l & 15) * 32 + 16);
      mx = fmaxf(fmaxf(fmaxf(a[0], a[1]), fmaxf(a[2], a[3])), fmaxf(fmaxf(b[0], b[1]), fmaxf(b[2], b[3])));
      const float p0 = __builtin_amdgcn_exp2f(s0[0] - mx), p1 = __builtin_amdgcn_exp2f(s0[1] - mx), p2 = __builtin_amdgcn_exp2f(s0[2] - mx),
                  p3 = __builtin_amdgcn_exp2f(s0[3] - mx);
      *(LAS v2u*)(S + A_P + (l & 15) * 256 + (((2 * w + (g >> 1)) ^ (l & 15)) << 4) + (g & 1) * 8) = (v2u){pk(p0, p1), pk(p2, p3)};
    }
    BAR();
    v4u pb[4];
#pragma unroll
    for (int ks = 0; ks < 4; ++ks) pb[ks] = ldsr(S + A_P + (l & 15) * 256 + (((4 * ks + g) ^ (l & 15)) << 4));
    if (w == 0) {
      f4 ls = {0.f, 0.f, 0.f, 0.f};
#pragma unroll
      for (int ks = 0; ks < 4; ++ks) ls = mfma32((v4u){0x3F803F80u, 0x3F803F80u, 0x3F803F80u, 0x3F803F80u}, pb[ks], ls);
      if (l < 16) ST8(((v2u){min(asu(mx), SENT - 1), min(asu(ls[0]), SENT - 1)}), W_ML + ((hg * 4 + (l >> 2)) * 32 + chunk) * 128 + (l & 3) * 8);
    }
    PIN();
    f4 o[4];
#pragma unroll
    for (int ct = 0; ct < 4; ++ct) o[ct] = mfma32(va[0][ct], pb[0], (f4){0.f, 0.f, 0.f, 0.f});
#pragma unroll
    for (int ks = 1; ks < 4; ++ks)
#pragma unroll
      for (int ct = 0; ct < 4; ++ct) o[ct] = mfma32(va[ks][ct], pb[ks], o[ct]);
    {
      LAS u8* tb = S + A_QA + w * 2048;
#pragma unroll
      for (int ct = 0; ct < 4; ++ct)
        *(LAS v2u*)(tb + (l & 15) * 128 + ((ct * 32 + g * 8) ^ ((l & 7) << 4))) = (v2u){min(pk(o[ct][0], o[ct][1]), SENT - 1), min(pk(o[ct][2], o[ct][3]), SENT - 1)};
      WAITLDS();
      const int hr = l >> 3;
      const v4u r0 = ldsr(tb + hr * 128 + (((l & 7) ^ hr) << 4)), r1 = ldsr(tb + 1024 + hr * 128 + (((l & 7) ^ hr) << 4));
      ST16(r0, opl0);
      ST16(r1, opl1);
    }
  } else {
    const int vi = bx * 8 + (br - 24), h = vi >> 1, dh = vi & 1;
    const int rr = l >> 3;
    const u32 uvo = (u32)((64 * w + rr) * 8192 + h * 256 + dh * 128) + (((l & 7) ^ (((rr >> 1) & 1) << 1)) << 4);
    ROLE_ARGS();
    ROLE_SMALL_LOADS();
    if (t == 0) *(volatile LAS u32*)(S + V_FLAG) = 0;
    {
      const int j = br - 24;
      const u8* p = (const u8*)ckv + (size_t)((4 * bx + (j >> 1)) * 128 + 32 + 64 * (j & 1) + w) * 1024;
#pragma unroll 1
      for (int i = 0; i < 4; ++i) { dma16(l * 16, p, lb + V_WUV + w * 8192 + i * 1024); p += 8192; }
    }
    if ((((u32)(size_t)wuv + h * 256 + dh * 128) >> 7 & 7) == 3) __builtin_amdgcn_s_sleep(V_DELAY_SLOW);
    else __builtin_amdgcn_s_sleep(V_DELAY);
    {
      const u8* p = (const u8*)wuv;
      const u8* po = (const u8*)wo + (size_t)(h * 128 + 64 * dh) * 1280;
#pragma unroll 1
      for (int i = 0; i < 10; ++i) {
        if (i < 8) { dma16(uvo ^ ((i & 1) << 6), p, lb + V_WUV + w * 8192 + i * 1024); p += 8 * 8192; }
        const u32 u = 512 * i + 64 * w + l, row = (u * 52429u) >> 22, pos = u - row * 80;
        const u32 sw = (row & 3) | (((row >> 3) & 1) << 2);
        dma16(row * 1280 + ((pos ^ (sw << 1)) << 4), po, lb + V_WO + (8 * i + w) * 1024);
      }
    }
    if (w == 7) dma16c(((h >> 4) * 32 + (l & 15) * 2) * 8, ws + W_TOKA, lb + V_TK);
    WAITVM(0);
    v4u b4[2][4], b5[5][2];
    {
      const u32 sw = ((q4 >> 1) & 1) | ((g & 1) << 1);
      LAS u8* b = S + V_WUV + (64 * w + 8 * g + q4) * 128 + p4 * 8;
#pragma unroll
      for (int ks = 0; ks < 2; ++ks)
#pragma unroll
        for (int ct = 0; ct < 4; ++ct) b4[ks][ct] = trd2(b + ks * 4096 + ((ct ^ sw) << 5), 512);
    }
    BAR();
    READ_TAG();
    v2u tk = *(LAS v2u*)(S + V_TK + (l & 31) * 8);
    {
      const u32 sw = q4 | ((g & 1) << 2);
      LAS u8* b = S + V_WO + (8 * g + q4) * 1280 + p4 * 8;
#pragma unroll
      for (int n = 0; n < 5; ++n)
#pragma unroll
        for (int ks = 0; ks < 2; ++ks) b5[n][ks] = trd2(b + ks * 32 * 1280 + (((5 * w + n) ^ sw) << 5), 4 * 1280);
    }
    if (!__all(tk.x == (u32)tag && tk.y == (u32)(tag >> 32)))
      _Pragma("nounroll") for (u32 spin = 0; spin < SPIN_CAP; ++spin) {
        RELOAD();
        tk = LD8(W_TOKA + ((h >> 4) * 32 + (l & 31)) * 8);
        if (__all(tk.x == (u32)tag && tk.y == (u32)(tag >> 32))) break;
      }
    {
      u32 opo[4];
#pragma unroll
      for (int i = 0; i < 4; ++i) {
        opo[i] = h * 32768 + ((8 * (8 * i + (l >> 3)) + w + 9 * h) & 255) * 128 + ((((l & 7) ^ ((l >> 4) & 3)) * 16) ^ ((i & 1) << 6));
        asm volatile("" : "+v"(opo[i]));
      }
      LAS u8* fa0 = S + V_WUV + w * 8192 + (l & 15) * 128 + ((g ^ (((l & 15) >> 1) & 7)) << 4);
      LAS u8* fa1 = S + V_WUV + w * 8192 + (l & 15) * 128 + (((4 + g) ^ (((l & 15) >> 1) & 7)) << 4);
      v2u ml;
      if (w == 0) __builtin_amdgcn_s_sleep(G_DELAY);
      if (w == 0)
        _Pragma("nounroll") for (u32 spin = 0; spin < SPIN_CAP; ++spin) {
          RELOAD();
          ml = LD8(W_ML + ((h >> 2) * 32 + (l & 31)) * 128 + (h & 3) * 8);
          if (__all(max(ml.x, ml.y) != SENT)) break;
        }
      __builtin_amdgcn_s_barrier();
      f4 y[2][4], wt0, wt1;
      u32 wflag;
      _Pragma("nounroll") for (u32 spin = 0; spin < SPIN_CAP; ++spin) {
#pragma unroll
        for (int i = 0; i < 4; ++i) dma16c(opo[i], ws + W_OP, lb + V_WUV + w * 8192 + i * 1024);
        if (spin == 0 && w == 0) {
          const float mm = asf(ml.x), gm = rows_max(row16_max(mm)), wt = __builtin_amdgcn_exp2f(mm - gm);
          const float lt = rows_sum(row16_sum(wt * asf(ml.y)));
          if (l < 32) *(volatile LAS float*)(S + V_O + l * 4) = wt * (2.f * __builtin_amdgcn_rcpf(lt));
          WAITLDS();
          *(volatile LAS u32*)(S + V_FLAG) = 1;
        }
        v4u v[2][2];
        WAITVM(2);
        v[0][0] = ldsr(fa0); v[0][1] = ldsr(fa1);
#pragma unroll
        for (int ct = 0; ct < 4; ++ct) y[0][ct] = mfma32(v[0][1], b4[1][ct], mfma32(v[0][0], b4[0][ct], (f4){0.f, 0.f, 0.f, 0.f}));
        PIN();
        WAITVM(0);
        v[1][0] = ldsr(fa0 + 2048); v[1][1] = ldsr(fa1 + 2048);
        wflag = *(volatile LAS u32*)(S + V_FLAG);
        wt0 = *(volatile LAS f4*)(S + V_O + g * 16);
        wt1 = *(volatile LAS f4*)(S + V_O + 64 + g * 16);
#pragma unroll
        for (int ct = 0; ct < 4; ++ct) y[1][ct] = mfma32(v[1][1], b4[1][ct], mfma32(v[1][0], b4[0][ct], (f4){0.f, 0.f, 0.f, 0.f}));
        if (__all(max(max(max4(v[0][0]), max4(v[0][1])), max(max4(v[1][0]), max4(v[1][1]))) != SENT)) break;
      }
      _Pragma("nounroll") for (u32 spin = 0; spin < SPIN_CAP && wflag != 1; ++spin) {
        wflag = *(volatile LAS u32*)(S + V_FLAG);
        wt0 = *(volatile LAS f4*)(S + V_O + g * 16);
        wt1 = *(volatile LAS f4*)(S + V_O + 64 + g * 16);
      }
      float cs[4];
#pragma unroll
      for (int ct = 0; ct < 4; ++ct) {
        cs[ct] = wt0[0] * y[0][ct][0];
#pragma unroll
        for (int j = 1; j < 4; ++j) cs[ct] = __builtin_fmaf(wt0[j], y[0][ct][j], cs[ct]);
#pragma unroll
        for (int j = 0; j < 4; ++j) cs[ct] = __builtin_fmaf(wt1[j], y[1][ct][j], cs[ct]);
      }
      const float cx = rows_sum_scatter(cs[0], cs[1], cs[2], cs[3]);
      const __bf16 hi = (__bf16)cx, lo = (__bf16)(cx - (float)hi);
      *(LAS __bf16*)(S + V_CP + w * 144 + l * 2) = hi;
      *(LAS __bf16*)(S + V_CP + (8 + w) * 144 + l * 2) = lo;
    }
    BAR();
    {
      const v4u a0 = ldsr(S + V_CP + (l & 15) * 144 + g * 16), a1 = ldsr(S + V_CP + (l & 15) * 144 + 64 + g * 16);
      float r[5];
#pragma unroll
      for (int n = 0; n < 5; ++n) {
        f4 acc = mfma32(a0, b5[n][0], (f4){0.f, 0.f, 0.f, 0.f});
        acc = mfma32(a1, b5[n][1], acc);
        r[n] = (acc[0] + acc[1]) + (acc[2] + acc[3]);
      }
      ST8(gran(tl24, thi, rows_sum_scatter(r[0], r[1], r[2], r[3])), W_PART + (((5 * w + g) * NPART + vi) * 16 + (l & 15)) * 8);
      const float r4 = rows_sum(r[4]);
      if (l < 16) ST8(gran(tl24, thi, r4), W_PART + (((5 * w + 4) * NPART + vi) * 16 + l) * 8);
    }
  }
}

extern "C" void kernel_launch(void* const* d_in, const int* in_sizes, int n_in, void* d_out, int out_size, void* d_ws, size_t ws_size,
                              hipStream_t stream) {
  gated_mix_mla_fused<<<256, 512, 0, stream>>>((const u16*)d_in[0], (const u16*)d_in[1], (const u16*)d_in[2], (const u16*)d_in[3],
                                               (const u16*)d_in[4], (const u16*)d_in[5], (const u16*)d_in[7], (const u16*)d_in[8],
                                               (const u16*)d_in[9], (u8*)d_ws, (u16*)d_out);
}
```
